```python
import torch

def _softmax(x, axis=-1):
    return torch.softmax(x, dim=axis)

def _gelu(x):
    return torch.nn.functional.gelu(x, approximate='tanh')

def _silu(x):
    return torch.nn.functional.silu(x)

def _sigmoid(x):
    return torch.sigmoid(x)

def _layernorm(x, axis=-1, eps=1e-05):
    m = x.mean(dim=axis, keepdim=True)
    v = x.var(dim=axis, keepdim=True, unbiased=False)
    return (x - m) / torch.sqrt(v + eps)

def _scatter_add(tgt, idx, src, axis=0):
    return tgt.clone().index_add_(axis, idx, src)

def _segment_sum(src, seg, n):
    out = torch.zeros((n,) + src.shape[1:], dtype=src.dtype)
    return out.index_add_(0, seg, src)

def _pad(x, pw):
    flat = tuple((v for pair in reversed(pw) for v in pair))
    return torch.nn.functional.pad(x, flat)
kernel_name = 'adaLN_cond__mha_block_cached__geglu_ffn__decode'

def setup_inputs():

    _w = lambda a: (lambda t: t.to(torch.bfloat16) if t.is_floating_point() else t)(torch.as_tensor(a))

    def _A_setup():
        B, Sq, D = (2, 1, 448)
        H, Sk, Dh = (16, 4096, 256)
        return {'x': _w((lambda *s: torch.randn(*s, dtype=torch.bfloat16) * 0.1)(B, Sq, D)), 'k_cache': _w((lambda *s: torch.randn(*s, dtype=torch.bfloat16) * 0.8)(B, H, Sk, Dh)), 'v_cache': _w((lambda *s: torch.randn(*s, dtype=torch.bfloat16) * 0.8)(B, H, Sk, Dh)), 'wq': _w((lambda *s: torch.randn(*s, dtype=torch.bfloat16) * 0.8)(D, H * Dh) * D ** (-0.5)), 'wo': _w((lambda *s: torch.randn(*s, dtype=torch.bfloat16) * 0.8)(H * Dh, D) * (H * Dh) ** (-0.5))}
    _A_in = _A_setup()

    def _B_setup():
        B, S, D, F = (2, 1, 448, 1792)
        return {'x': _w((lambda *s: torch.randn(*s, dtype=torch.bfloat16) * 0.1)(B, S, D)), 'w_gate': _w((lambda *s: torch.randn(*s, dtype=torch.bfloat16) * 0.1)(D, F)), 'w_up': _w((lambda *s: torch.randn(*s, dtype=torch.bfloat16) * 0.1)(D, F)), 'w_down': _w((lambda *s: torch.randn(*s, dtype=torch.bfloat16) * 0.1)(F, D))}
    _B_in = _B_setup()
    return {'A_x': _A_in['x'], 'A_k_cache': _A_in['k_cache'], 'A_v_cache': _A_in['v_cache'], 'A_wq': _A_in['wq'], 'A_wo': _A_in['wo'], 'B_x': _B_in['x'], 'B_w_gate': _B_in['w_gate'], 'B_w_up': _B_in['w_up'], 'B_w_down': _B_in['w_down'], 'c': _w((lambda *s: torch.randn(*s, dtype=torch.bfloat16) * 0.8)(2, 448) * 8.0), 'w_mod': _w((lambda *s: torch.randn(*s, dtype=torch.bfloat16) * 0.8)(448, 6 * 448) * 448 ** (-0.5) * 8.0)}

def reference(A_x, A_k_cache, A_v_cache, A_wq, A_wo, B_x, B_w_gate, B_w_up, B_w_down, c, w_mod):

    def _A_ref(x, k_cache, v_cache, wq, wo):
        B, Sq, D = x.shape
        H, Dh = (16, 256)
        q = (lambda x, *d: x.permute(*d))((x @ wq).reshape(B, Sq, H, Dh), 0, 2, 1, 3)
        scores = torch.einsum('bhqd,bhkd->bhqk', q, k_cache) * Dh ** (-0.5)
        p = _softmax(scores, axis=-1)
        o = torch.einsum('bhqk,bhkd->bhqd', p, v_cache)
        return (lambda x, *d: x.permute(*d))(o, 0, 2, 1, 3).reshape(B, Sq, H * Dh) @ wo

    def _B_ref(x, w_gate, w_up, w_down):
        return _gelu(x @ w_gate) * (x @ w_up) @ w_down
    x = A_x
    mod = (_silu(c) @ w_mod).reshape(c.shape[0], 6, 1, -1)
    sa, ca, ga, sb, cb, gb = (mod[:, 0].clone(), mod[:, 1].clone(), mod[:, 2].clone(), mod[:, 3].clone(), mod[:, 4].clone(), mod[:, 5].clone())
    h = _layernorm(x, axis=-1) * (1 + ca) + sa
    x = x + ga * _A_ref(h, A_k_cache, A_v_cache, A_wq, A_wo)
    h = _layernorm(x, axis=-1) * (1 + cb) + sb
    x = x + gb * _B_ref(h, B_w_gate, B_w_up, B_w_down)
    return x
```

```cpp
#include <hip/hip_runtime.h>
#include <hip/hip_bf16.h>
#include <stdint.h>

typedef unsigned short u16;
constexpr int B = 2, D = 448, H = 16, SK = 4096, DH = 256, F = 1792, NM = 6 * D;
constexpr int NS = 8;

__device__ __forceinline__ float bf2f(u16 v) { return __uint_as_float(((unsigned)v) << 16); }
__device__ __forceinline__ u16 f2bf(float f) { __hip_bfloat16 h = __float2bfloat16(f); return *reinterpret_cast<u16*>(&h); }
__device__ __forceinline__ float rbf(float f) { return f; }

__global__ void k_mod(const u16* c, const u16* wmod, float* mod) {
  int n = blockIdx.x * blockDim.x + threadIdx.x;
  if (n >= NM) return;
  float a0 = 0.f, a1 = 0.f;
  for (int k = 0; k < D; ++k) {
    float c0 = bf2f(c[k]), c1 = bf2f(c[D + k]);
    float s0 = rbf(c0 / (1.f + expf(-c0))), s1 = rbf(c1 / (1.f + expf(-c1)));
    float w = bf2f(wmod[(size_t)k * NM + n]);
    a0 = fmaf(s0, w, a0); a1 = fmaf(s1, w, a1);
  }
  mod[n] = rbf(a0); mod[NM + n] = rbf(a1);
}

__device__ void ln_mod(const float* x  , const float* shift, const float* scale, float* h, float* red) {
  int t = threadIdx.x;
  if (t == 0) {
    float s = 0.f; for (int i = 0; i < D; ++i) s += x[i];
    float mean = s / D;
    float v = 0.f; for (int i = 0; i < D; ++i) { float d = x[i] - mean; v += d * d; }
    v /= D;
    red[0] = rbf(mean); red[1] = rbf(sqrtf(rbf(rbf(v) + 1e-5f)));
  }
  __syncthreads();
  for (int i = t; i < D; i += blockDim.x) {
    float d = rbf(x[i] - red[0]);
    float n = rbf(d / red[1]);
    float y = rbf(n * rbf(1.f + scale[i]));
    h[i] = rbf(y + shift[i]);
  }
  __syncthreads();
}

__global__ void k_q(const u16* x, const float* mod, const u16* wq, float* q) {
  __shared__ float xs[D], h[D], red[2];
  int b = blockIdx.y;
  for (int i = threadIdx.x; i < D; i += blockDim.x) xs[i] = bf2f(x[b * D + i]);
  __syncthreads();
  ln_mod(xs, mod + b * NM + 0 * D, mod + b * NM + 1 * D, h, red);
  int n = blockIdx.x * blockDim.x + threadIdx.x;
  float a = 0.f;
  for (int k = 0; k < D; ++k) a = fmaf(h[k], bf2f(wq[(size_t)k * (H * DH) + n]), a);
  q[b * H * DH + n] = rbf(a);
}

__global__ void k_attn(const float* q, const u16* K, const u16* V, float* po, float* pml) {
  __shared__ float qs[DH], sc[SK / NS], red[256];
  int s = blockIdx.x, h = blockIdx.y, b = blockIdx.z, t = threadIdx.x;
  qs[t] = q[(b * H + h) * DH + t];
  __syncthreads();
  const int NK = SK / NS;
  const u16* Kb = K + ((size_t)(b * H + h) * SK + s * NK) * DH;
  const u16* Vb = V + ((size_t)(b * H + h) * SK + s * NK) * DH;
  float mx = -INFINITY;
  for (int k = t; k < NK; k += 256) {
    float a = 0.f;
    for (int d = 0; d < DH; ++d) a = fmaf(qs[d], bf2f(Kb[(size_t)k * DH + d]), a);
    a = rbf(a) * 0.0625f;
    sc[k] = a; mx = fmaxf(mx, a);
  }
  red[t] = mx; __syncthreads();
  for (int o = 128; o > 0; o >>= 1) { if (t < o) red[t] = fmaxf(red[t], red[t + o]); __syncthreads(); }
  mx = red[0]; __syncthreads();
  float l = 0.f;
  for (int k = t; k < NK; k += 256) { float p = expf(sc[k] - mx); sc[k] = p; l += p; }
  red[t] = l; __syncthreads();
  for (int o = 128; o > 0; o >>= 1) { if (t < o) red[t] += red[t + o]; __syncthreads(); }
  l = red[0];
  float a = 0.f;
  for (int k = 0; k < NK; ++k) a = fmaf(sc[k], bf2f(Vb[(size_t)k * DH + t]), a);
  int slot = (b * H + h) * NS + s;
  po[slot * DH + t] = a;
  if (t == 0) { pml[slot * 2] = mx; pml[slot * 2 + 1] = l; }
}

__global__ void k_comb(const float* po, const float* pml, float* o) {
  int bh = blockIdx.x, t = threadIdx.x;
  float M = -INFINITY;
  for (int s = 0; s < NS; ++s) M = fmaxf(M, pml[(bh * NS + s) * 2]);
  float L = 0.f, a = 0.f;
  for (int s = 0; s < NS; ++s) { float w = expf(pml[(bh * NS + s) * 2] - M); L += w * pml[(bh * NS + s) * 2 + 1]; a += w * po[(bh * NS + s) * DH + t]; }
  o[bh * DH + t] = rbf(a / L);
}

__global__ void k_wo(const float* o, const u16* wo, const u16* x, const float* mod, float* x1) {
  int b = blockIdx.y, n = blockIdx.x * 64 + threadIdx.x;
  float a = 0.f;
  for (int k = 0; k < H * DH; ++k) a = fmaf(o[b * H * DH + k], bf2f(wo[(size_t)k * D + n]), a);
  float y = rbf(a);
  x1[b * D + n] = rbf(bf2f(x[b * D + n]) + rbf(mod[b * NM + 2 * D + n] * y));
}

__global__ void k_ffn1(const float* x1, const float* mod, const u16* wg, const u16* wu, float* act) {
  __shared__ float xs[D], h[D], red[2];
  int b = blockIdx.y;
  for (int i = threadIdx.x; i < D; i += blockDim.x) xs[i] = x1[b * D + i];
  __syncthreads();
  ln_mod(xs, mod + b * NM + 3 * D, mod + b * NM + 4 * D, h, red);
  int n = blockIdx.x * blockDim.x + threadIdx.x;
  float g = 0.f, u = 0.f;
  for (int k = 0; k < D; ++k) { g = fmaf(h[k], bf2f(wg[(size_t)k * F + n]), g); u = fmaf(h[k], bf2f(wu[(size_t)k * F + n]), u); }
  g = rbf(g); u = rbf(u);
  float ge = 0.5f * g * (1.f + tanhf(0.7978845608028654f * (g + 0.044715f * g * g * g)));
  act[b * F + n] = rbf(rbf(ge) * u);
}

__global__ void k_ffn2(const float* act, const u16* wd, const float* x1, const float* mod, u16* out) {
  int b = blockIdx.y, n = blockIdx.x * 64 + threadIdx.x;
  float a = 0.f;
  for (int k = 0; k < F; ++k) a = fmaf(act[b * F + k], bf2f(wd[(size_t)k * D + n]), a);
  float y = rbf(a);
  out[b * D + n] = f2bf(x1[b * D + n] + rbf(mod[b * NM + 5 * D + n] * y));
}

extern "C" void kernel_launch(void* const* d_in, const int* in_sizes, int n_in,
                              void* d_out, int out_size, void* d_ws, size_t ws_size,
                              hipStream_t stream) {
  const u16* x = (const u16*)d_in[0];
  const u16* K = (const u16*)d_in[1];
  const u16* V = (const u16*)d_in[2];
  const u16* wq = (const u16*)d_in[3];
  const u16* wo = (const u16*)d_in[4];
  const u16* wg = (const u16*)d_in[6];
  const u16* wu = (const u16*)d_in[7];
  const u16* wd = (const u16*)d_in[8];
  const u16* c = (const u16*)d_in[9];
  const u16* wmod = (const u16*)d_in[10];
  float* ws = (float*)d_ws;
  float* mod = ws;
  float* q = mod + 8192;
  float* po = q + 8192;
  float* pml = po + 65536;
  float* o = pml + 1024;
  float* x1 = o + 8192;
  float* act = x1 + 1024;
  k_mod<<<(NM + 63) / 64, 64, 0, stream>>>(c, wmod, mod);
  k_q<<<dim3(H * DH / 64, B), 64, 0, stream>>>(x, mod, wq, q);
  k_attn<<<dim3(NS, H, B), 256, 0, stream>>>(q, K, V, po, pml);
  k_comb<<<B * H, 256, 0, stream>>>(po, pml, o);
  k_wo<<<dim3(7, B), 64, 0, stream>>>(o, wo, x, mod, x1);
  k_ffn1<<<dim3(F / 64, B), 64, 0, stream>>>(x1, mod, wg, wu, act);
  k_ffn2<<<dim3(7, B), 64, 0, stream>>>(act, wd, x1, mod, (u16*)d_out);
}
```

```cpp
#include <hip/hip_runtime.h>
#include <stdint.h>

typedef unsigned u32;
typedef unsigned short u16;
typedef u32 u32x4 __attribute__((ext_vector_type(4)));
typedef u32 u32x2 __attribute__((ext_vector_type(2)));
typedef float f32x4 __attribute__((ext_vector_type(4)));
typedef float f32x2 __attribute__((ext_vector_type(2)));
typedef __bf16 bf16x2 __attribute__((ext_vector_type(2)));
typedef short s16x4 __attribute__((ext_vector_type(4)));
typedef short s16x8 __attribute__((ext_vector_type(8)));
typedef __bf16 bf16x8 __attribute__((ext_vector_type(8)));
typedef __amdgpu_buffer_rsrc_t rsrc_t;
#define LAS __attribute__((address_space(3)))

namespace {

constexpr int D = 448, NH = 16, SK = 4096, DH = 256, FF = 1792, NM = 6 * D;
constexpr int NWG = 256, NTHR = 512;
#define CLSEXPR (spl < 6 ? 1 : 0)
#define RING_ 56
#define WA_ 8
#define WB_ 6
#define NSA_ 72
#define NSB_ 68
#define IMGDELAY_ 450
#define QSLEEP_ 500
constexpr int NS = 72;
constexpr int RING = RING_;
constexpr int WA = WA_, WB = WB_;
constexpr int NSA = NSA_, NSB = NSB_, NSQ = (512 - 3 * NSA - 3 * NSB) / 2;
static_assert(3 * NSA + 3 * NSB + 2 * NSQ == 512 && NSA <= NS && NSB <= NS && NSQ <= NS && NSQ > 0, "");
constexpr int G_RED = 0, N_RED = 56;
constexpr int G_FFN = 96, N_FFN = 56;
constexpr int G_MOD = 152, N_MOD = 28;
constexpr int G_QP = 192, N_QP = 64;

constexpr int WS_EPOCH = 0;
constexpr int WS_MOD = 4096;
constexpr int WS_MU = 25600;
constexpr int WS_Q = 32768;
constexpr int WS_ST = WS_Q + 2 * 1024 * 16;
constexpr int WS_X1 = WS_ST + 256 * 128;
constexpr int WS_O = WS_X1 + 8192;
constexpr int WS_Y = WS_O + 256 * 128 * 16;
constexpr int WS_F = WS_Y + 256 * 224 * 16;
constexpr int WS_END = WS_F + N_FFN * 2 * 224 * 16;
constexpr int WS_STAMP = 8 << 20;

constexpr int AUX_NT = 2, AUX_SC1 = 16;
constexpr u32 SPIN_CAP = 1u << 22;

constexpr int L_FLAG = 0;
constexpr int L_ML = 64;
constexpr int L_AUX = 128;
constexpr int L_Q = 256;
constexpr int L_P = L_Q + 512;
constexpr int L_O = L_P + 8 * NS * 4;
constexpr int L_H = L_O + 4096;
constexpr int L_H2 = L_H + 2048;
constexpr int L_RED = L_H2 + 2048;
constexpr int L_ACT = L_RED + 1024;
constexpr int L_LN = L_ACT + 256;
constexpr int L_W = L_LN + 3584;
constexpr int L_W2 = L_W + 448 * 128;
constexpr int L_END = L_W2 + 32 * 896;
static_assert(L_END <= 160 * 1024 && (L_O % 16) == 0 && (L_W % 16) == 0, "");

__device__ __forceinline__ rsrc_t mk_rsrc(const void* p, u32 bytes) {
  return __builtin_amdgcn_make_buffer_rsrc((void*)p, 0, (int)bytes, 0x00020000);
}
template <int AUX> __device__ __forceinline__ u32x4 bld128(rsrc_t r, int voff, int soff) {
  return __builtin_amdgcn_raw_buffer_load_b128(r, voff, soff, AUX);
}
template <int AUX> __device__ __forceinline__ u32x2 bld64(rsrc_t r, int voff, int soff) {
  return __builtin_amdgcn_raw_buffer_load_b64(r, voff, soff, AUX);
}
template <int AUX> __device__ __forceinline__ u32 bld32(rsrc_t r, int voff, int soff) {
  return __builtin_amdgcn_raw_buffer_load_b32(r, voff, soff, AUX);
}
__device__ __forceinline__ void bst128_sc1(rsrc_t r, int voff, u32x4 v) {
  __builtin_amdgcn_raw_buffer_store_b128(v, r, voff, 0, AUX_SC1);
}
__device__ __forceinline__ float dot2(u32 a, u32 b, float c) {
  return __builtin_amdgcn_fdot2_f32_bf16(__builtin_bit_cast(bf16x2, a), __builtin_bit_cast(bf16x2, b), c, false);
}
__device__ __forceinline__ u32 pack2(float lo, float hi) {
  bf16x2 v = {(__bf16)lo, (__bf16)hi};
  return __builtin_bit_cast(u32, v);
}
__device__ __forceinline__ float rbf(float f) { return (float)(__bf16)f; }
__device__ __forceinline__ float blo(u32 v) { return __uint_as_float(v << 16); }
__device__ __forceinline__ float bhi(u32 v) { return __uint_as_float(v & 0xffff0000u); }
__device__ __forceinline__ u32 perm_lo(u32 a, u32 b) { return __builtin_amdgcn_perm(b, a, 0x05040100u); }
__device__ __forceinline__ u32 perm_hi(u32 a, u32 b) { return __builtin_amdgcn_perm(b, a, 0x07060302u); }
template <int CTRL> __device__ __forceinline__ float dpp(float x) {
  return __builtin_bit_cast(float, __builtin_amdgcn_mov_dpp(__builtin_bit_cast(int, x), CTRL, 0xf, 0xf, true));
}
template <int CTRL> __device__ __forceinline__ u32 dppu(u32 x) {
  return (u32)__builtin_amdgcn_mov_dpp((int)x, CTRL, 0xf, 0xf, true);
}
constexpr int XOR1 = 0xB1, XOR2 = 0x4E, XOR7 = 0x141, ROR8 = 0x128, ROR4 = 0x124, ROR2 = 0x122, ROR1 = 0x121;
__device__ __forceinline__ float tr32(float a, float b) {
  auto s = __builtin_amdgcn_permlane32_swap(__float_as_uint(a), __float_as_uint(b), false, false);
  return __uint_as_float(s[0]) + __uint_as_float(s[1]);
}
__device__ __forceinline__ float tr16(float a, float b) {
  auto s = __builtin_amdgcn_permlane16_swap(__float_as_uint(a), __float_as_uint(b), false, false);
  return __uint_as_float(s[0]) + __uint_as_float(s[1]);
}
__device__ __forceinline__ float max16(float x) {
  auto s = __builtin_amdgcn_permlane16_swap(__float_as_uint(x), __float_as_uint(x), false, false);
  return fmaxf(__uint_as_float(s[0]), __uint_as_float(s[1]));
}
__device__ __forceinline__ float max32(float x) {
  auto s = __builtin_amdgcn_permlane32_swap(__float_as_uint(x), __float_as_uint(x), false, false);
  return fmaxf(__uint_as_float(s[0]), __uint_as_float(s[1]));
}
__device__ __forceinline__ float row_sum(float x) {
  x += dpp<ROR8>(x); x += dpp<ROR4>(x); x += dpp<ROR2>(x); x += dpp<ROR1>(x);
  return x;
}
__device__ __forceinline__ float wave_sum(float x) { x = row_sum(x); x = tr16(x, x); return tr32(x, x); }
__device__ __forceinline__ float wave_max(float x) {
  x = fmaxf(x, dpp<ROR8>(x)); x = fmaxf(x, dpp<ROR4>(x)); x = fmaxf(x, dpp<ROR2>(x)); x = fmaxf(x, dpp<ROR1>(x));
  return max32(max16(x));
}
constexpr int vmcnt_imm(int n) { return (n & 15) | ((n >> 4) << 14) | (7 << 4) | (15 << 8); }
#define WAIT_VM(n) __builtin_amdgcn_s_waitcnt(vmcnt_imm(n))
#define WAIT_LGKM0() __builtin_amdgcn_s_waitcnt(0xC07F)
#define SCHED() __builtin_amdgcn_sched_barrier(0)
#define CBAR() asm volatile("" ::: "memory")
#define LDS_FENCE() do { CBAR(); WAIT_LGKM0(); CBAR(); } while (0)
#define PIN2(a, b) asm volatile("" : "+v"(a), "+v"(b))
__device__ __forceinline__ void wg_barrier() { LDS_FENCE(); __builtin_amdgcn_s_barrier(); CBAR(); }

#define STAMP(i) do { } while (0)

template <int N, int SLEEP = 0>
__device__ __forceinline__ void poll(rsrc_t ws, const int (&voff)[N], const bool (&act)[N], u32 tag, u32x4 (&r)[N]) {
  bool ok[N];
#pragma unroll
  for (int i = 0; i < N; ++i) { ok[i] = !act[i]; r[i] = u32x4{0u, 0u, 0u, 0u}; }
  for (u32 spin = 0; spin < SPIN_CAP; ++spin) {
    CBAR();
#pragma unroll
    for (int i = 0; i < N; ++i) if (!ok[i]) r[i] = bld128<AUX_SC1>(ws, voff[i], 0);
    bool all = true;
#pragma unroll
    for (int i = 0; i < N; ++i) { ok[i] = ok[i] || (r[i].y == tag && r[i].w == tag); all = all && ok[i]; }
    if (__all(all)) break;
    if (SLEEP) __builtin_amdgcn_s_sleep(SLEEP);
  }
  CBAR();
}
__device__ __forceinline__ void lds_wait(volatile LAS u32* f, u32 want = 1u) {
  for (u32 spin = 0; spin < SPIN_CAP; ++spin) {
    if (*f >= want) break;
    __builtin_amdgcn_s_sleep(1);
  }
  CBAR();
}
__device__ __forceinline__ u32 lds_inc(LAS unsigned char* L, int idx) {
  return __hip_atomic_fetch_add((LAS u32*)(L + L_FLAG) + idx, 1u, __ATOMIC_RELAXED, __HIP_MEMORY_SCOPE_WORKGROUP);
}
__device__ __forceinline__ void aux_sync(LAS unsigned char* L, int idx, int lane) {
  LDS_FENCE();
  if (lane == 0) lds_inc(L, idx);
  volatile LAS u32* f = (volatile LAS u32*)(L + L_FLAG) + idx;
  for (u32 spin = 0; spin < SPIN_CAP; ++spin) if (*f >= 4u) break;
  CBAR();
}

__device__ __forceinline__ void ln_stats(const float (&x)[8], bool act1, float& mean_b, float& rsd) {
  float s = (x[0] + x[1]) + (x[2] + x[3]);
  float s1 = (x[4] + x[5]) + (x[6] + x[7]);
  s += act1 ? s1 : 0.f;
  const float mean = wave_sum(s) * (1.0f / D);
  float v = 0.f, v1 = 0.f;
#pragma unroll
  for (int i = 0; i < 4; ++i) { float d = x[i] - mean; v = fmaf(d, d, v); float e = x[4 + i] - mean; v1 = fmaf(e, e, v1); }
  v += act1 ? v1 : 0.f;
  const float var = wave_sum(v) * (1.0f / D);
  mean_b = rbf(mean);
  rsd = __builtin_amdgcn_rcpf(rbf(__builtin_amdgcn_sqrtf(rbf(rbf(var) + 1e-5f))));
}
__device__ __forceinline__ float ln_one(float x, float mean_b, float rsd, float sh, float sc) {
  const float n = rbf(rbf(x - mean_b) * rsd);
  return rbf(n * rbf(1.f + sc)) + sh;
}

template <int I> struct ic { static constexpr int value = I; };
template <int B, int E, class F> __device__ __forceinline__ void static_for(F&& f) {
  if constexpr (B < E) { f(ic<B>{}); static_for<B + 1, E>(f); }
}
constexpr int cons_upto(int t) {
  if (t < 0) return 0;
  if (RING + t >= 2 * NS) return 2 * NS;
  int a = 4 * (t + 1), b = RING + t + 1 - WA;
  return a < b ? a : b;
}
#define PACE() do { WAIT_VM(WA - 1); if (WB < WA && cls != 0) WAIT_VM(WB - 1); } while (0)

__device__ __forceinline__ void dma16(const void* src, u32 lds_dst) {
  unsigned keep;
  asm volatile("s_mov_b32 %0, m0\n\ts_mov_b32 m0, %2\n\ts_nop 0\n\tglobal_load_lds_dwordx4 %1, off nt\n\ts_mov_b32 m0, %0"
               : "=&s"(keep) : "v"(src), "s"(lds_dst) : "memory");
}
__device__ __forceinline__ u32 ald32(const void* base, int voff) {
  u32 r; asm volatile("global_load_dword %0, %1, %2 nt" : "=v"(r) : "v"(voff), "s"(base) : "memory"); return r;
}
__device__ __forceinline__ u32 ald32_sc1(const void* base, int voff) {
  u32 r; asm volatile("global_load_dword %0, %1, %2 sc1" : "=v"(r) : "v"(voff), "s"(base) : "memory"); return r;
}
__device__ __forceinline__ u32x2 ald64(const void* base, int voff) {
  u32x2 r; asm volatile("global_load_dwordx2 %0, %1, %2 nt" : "=v"(r) : "v"(voff), "s"(base) : "memory"); return r;
}
template <int PITCH>
__device__ __forceinline__ void load_afrags(LAS unsigned char* img, int lane, s16x8 (&af)[14]) {
  LAS unsigned char* p = img + (8 * (lane >> 4) + ((lane & 15) >> 2)) * PITCH + (lane & 3) * 8;
#pragma unroll
  for (int s = 0; s < 14; ++s) {
    const s16x4 lo = __builtin_amdgcn_ds_read_tr16_b64_v4i16((LAS s16x4*)(p + s * 32 * PITCH));
    const s16x4 hi = __builtin_amdgcn_ds_read_tr16_b64_v4i16((LAS s16x4*)(p + s * 32 * PITCH + 4 * PITCH));
    af[s] = __builtin_shufflevector(lo, hi, 0, 1, 2, 3, 4, 5, 6, 7);
    asm volatile("" : "+v"(af[s]));
  }
}
__device__ __forceinline__ f32x4 gemv_tile(LAS unsigned char* h, int lane, const s16x8 (&af)[14]) {
  const LAS u32x4* hp = (const LAS u32x4*)(h + (lane & 1) * 1024 + (lane >> 4) * 16);
  u32x4 bf[14];
#pragma unroll
  for (int s = 0; s < 14; ++s) bf[s] = hp[s * 4];
  f32x4 acc0 = {0.f, 0.f, 0.f, 0.f}, acc1 = {0.f, 0.f, 0.f, 0.f};
#pragma unroll
  for (int s = 0; s < 14; s += 2) {
    acc0 = __builtin_amdgcn_mfma_f32_16x16x32_bf16(__builtin_bit_cast(bf16x8, af[s]), __builtin_bit_cast(bf16x8, bf[s]), acc0, 0, 0, 0);
    acc1 = __builtin_amdgcn_mfma_f32_16x16x32_bf16(__builtin_bit_cast(bf16x8, af[s + 1]), __builtin_bit_cast(bf16x8, bf[s + 1]), acc1, 0, 0, 0);
  }
  return acc0 + acc1;
}
__device__ __forceinline__ void load_wd(LAS unsigned char* L, int dq, int lane, u32 (&wd)[32]) {
  const int l56 = lane < 56 ? lane : 0;
  const LAS u32* wp = (const LAS u32*)(L + L_W2 + (112 * dq + 2 * l56) * 2);
#pragma unroll
  for (int p = 0; p < 16; ++p) {
    const u32 ra = wp[(2 * p) * 224], rb = wp[(2 * p + 1) * 224];
    wd[2 * p] = perm_lo(ra, rb); wd[2 * p + 1] = perm_hi(ra, rb);
    PIN2(wd[2 * p], wd[2 * p + 1]);
  }
}

}

__global__ void __launch_bounds__(NTHR, 2)
fused_decode_kernel(const void* __restrict__ px, const void* __restrict__ pK, const void* __restrict__ pV,
                    const void* __restrict__ pwq, const void* __restrict__ pwo, const void* __restrict__ pwg,
                    const void* __restrict__ pwu, const void* __restrict__ pwd, const void* __restrict__ pc,
                    const void* __restrict__ pwmod, void* __restrict__ pout, void* ws) {
  __shared__ __attribute__((aligned(16))) unsigned char lds_raw[L_END];
  LAS unsigned char* L = (LAS unsigned char*)lds_raw;
  const int tid = threadIdx.x, lane = tid & 63;
  const int wave = __builtin_amdgcn_readfirstlane(tid >> 6);
  const int g = blockIdx.x;
  const int head = 2 * (g & 7) + ((g >> 3) & 1), bat = (g >> 4) & 1, spl = g >> 5;
  const int bh = bat * NH + head;
  const bool is_red = g < G_RED + N_RED, is_ffn = g >= G_FFN && g < G_FFN + N_FFN;
  const bool is_mod = g >= G_MOD && g < G_MOD + N_MOD, is_qp = g >= G_QP;
  const bool has_img = is_mod || is_qp || is_ffn;
  const int fi = g - G_FFN;
  const int fcg = fi < 48 ? (fi >> 4) * 16 + (fi & 7) * 2 + ((fi >> 3) & 1) : fi;
  const rsrc_t rws = mk_rsrc(ws, WS_END);
  volatile LAS u32* flags = (volatile LAS u32*)(L + L_FLAG);
  const u32 lds0 = (u32)(uintptr_t)L;
  STAMP(0);
  u32 tag;
  u32 wd[32];
  s16x8 af[14];

  if (wave < 4) {
    const int ns = spl < 3 ? NSA : spl < 6 ? NSB : NSQ;
    const int s0 = spl < 3 ? spl * NSA : spl < 6 ? 3 * NSA + (spl - 3) * NSB : 3 * NSA + 3 * NSB + (spl - 6) * NSQ;
    const int cls = CLSEXPR;
    const size_t chunk = ((size_t)bh * SK) * (DH * 2) + (size_t)s0 * 4096;
    const rsrc_t rK = mk_rsrc((const char*)pK + chunk, ns * 4096);
    const rsrc_t rV = mk_rsrc((const char*)pV + chunk, ns * 4096);
    const int voff = wave * 1024 + lane * 16;
    const int shift = NS - ns;
    const int voffv = voff - shift * 4096;
    const int half = lane >> 5, c = lane & 31;
    LAS float* P = (LAS float*)(L + L_P) + (wave * 2 + half) * NS;
    P[c] = 0.f;
    static_assert(NS - NSQ <= 32 && NS - NSB <= 32 && NSQ >= 32, "");
    u32x4 kv[2 * NS];
    wg_barrier();
    if (is_qp) lds_wait(flags + 5);
    SCHED();
    STAMP(5);
#pragma unroll
    for (int i = 0; i < RING; ++i) {
      if (i >= WB) PACE();
      kv[i] = bld128<AUX_NT>(rK, voff + i * 4096, 0);
      SCHED();
    }
    STAMP(1);
    lds_wait(flags + 0);
    STAMP(2);
    tag = flags[2];
    const u32x4 q = *(const LAS u32x4*)(L + L_Q + c * 16);
    float lvl[5];
    float sc[3];
    f32x2 acc[4];
#pragma unroll
    for (int k = 0; k < 4; ++k) acc[k] = f32x2{0.f, 0.f};
    float m_w = 0.f, scale = 0.f;
    f32x4 pv = {0.f, 0.f, 0.f, 0.f};
    static_for<0, 2 * NS - RING + 1>([&](auto tc) {
      constexpr int t = decltype(tc)::value;
      constexpr int e = RING + t;
      if constexpr (e < 2 * NS) {
        PACE();
        if constexpr (e < NS) kv[e] = bld128<AUX_NT>(rK, voff + e * 4096, 0);
        else kv[e] = bld128<AUX_NT>(rV, voffv + (e - NS) * 4096, 0);
        SCHED();
      }
      if constexpr (e == 2 * NS - 6) {
        lds_wait(flags + 1, 4u);
        const LAS f32x2* ml = (const LAS f32x2*)(L + L_ML);
        const float M = fmaxf(fmaxf(ml[0].x, ml[1].x), fmaxf(ml[2].x, ml[3].x));
        scale = __expf(m_w - M);
        asm volatile("" : "+v"(scale));
      }
      static_for<cons_upto(t - 1), cons_upto(t)>([&](auto icn) {
        constexpr int i = decltype(icn)::value;
        if constexpr (i < NS) {
          float d = dot2(kv[i].x, q.x, 0.f);
          d = dot2(kv[i].y, q.y, d); d = dot2(kv[i].z, q.z, d); d = dot2(kv[i].w, q.w, d);
          constexpr int j = i & 31;
          if constexpr ((j & 1) == 0) lvl[0] = d;
          else {
            float x = tr16(lvl[0], d);
            if constexpr ((j & 2) == 0) lvl[1] = x;
            else {
              float a = lvl[1] + dpp<ROR8>(lvl[1]), b = x + dpp<ROR8>(x);
              x = (lane & 8) ? b : a;
              if constexpr ((j & 4) == 0) lvl[2] = x;
              else {
                a = lvl[2] + dpp<XOR7>(lvl[2]); b = x + dpp<XOR7>(x);
                x = (lane & 4) ? b : a;
                if constexpr (i == 71) { x += dpp<XOR2>(x); sc[2] = x + dpp<XOR1>(x); }
                else if constexpr ((j & 8) == 0) lvl[3] = x;
                else {
                  a = lvl[3] + dpp<XOR2>(lvl[3]); b = x + dpp<XOR2>(x);
                  x = (lane & 2) ? b : a;
                  if constexpr ((j & 16) == 0) lvl[4] = x;
                  else {
                    a = lvl[4] + dpp<XOR1>(lvl[4]); b = x + dpp<XOR1>(x);
                    sc[i >> 5] = (lane & 1) ? b : a;
                  }
                }
              }
            }
          }
          if constexpr (i == NS - 1) {
            const int sl = ((lane >> 4) & 1) | ((lane >> 3) & 1) << 1 | ((lane >> 2) & 1) << 2 | ((lane >> 1) & 1) << 3 | (lane & 1) << 4;
            const bool v1 = 32 + sl < ns, v2 = sl < 8 && 64 + sl < ns;
            const float t0 = rbf(sc[0]) * 0.0625f;
            const float t1 = v1 ? rbf(sc[1]) * 0.0625f : -INFINITY, t2 = v2 ? rbf(sc[2]) * 0.0625f : -INFINITY;
            m_w = wave_max(fmaxf(fmaxf(t0, t1), t2));
            const float p0 = __expf(t0 - m_w), p1 = __expf(t1 - m_w), p2 = __expf(t2 - m_w);
            const float l_w = wave_sum((p0 + p1) + p2);
            P[shift + sl] = p0;
            if (v1) P[shift + 32 + sl] = p1;
            if (v2) P[shift + 64 + sl] = p2;
            if (lane == 0) *(LAS f32x2*)(L + L_ML + wave * 8) = f32x2{m_w, l_w};
            LDS_FENCE();
            u32 old = 0;
            if (lane == 0) old = lds_inc(L, 1);
            old = __builtin_amdgcn_readfirstlane(old);
            if (old == 3u) {
              CBAR();
              const LAS f32x2* ml = (const LAS f32x2*)(L + L_ML);
              const f32x2 a0 = ml[0], a1 = ml[1], a2 = ml[2], a3 = ml[3];
              const float M = fmaxf(fmaxf(a0.x, a1.x), fmaxf(a2.x, a3.x));
              const float Lw = (a0.y * __expf(a0.x - M) + a1.y * __expf(a1.x - M)) + (a2.y * __expf(a2.x - M) + a3.y * __expf(a3.x - M));
              if (lane == 0) bst128_sc1(rws, WS_ST + (bh * 8 + spl) * 128, u32x4{__float_as_uint(M), tag, __float_as_uint(Lw), tag});
            }
            STAMP(3);
          }
        } else {
          constexpr int j = i - NS;
          if constexpr ((j & 3) == 0) pv = *(const LAS f32x4*)(P + j);
          const float p = pv[j & 3];
          const f32x2 pp = {p, p};
          acc[0] = __builtin_elementwise_fma(pp, f32x2{blo(kv[i].x), bhi(kv[i].x)}, acc[0]);
          acc[1] = __builtin_elementwise_fma(pp, f32x2{blo(kv[i].y), bhi(kv[i].y)}, acc[1]);
          acc[2] = __builtin_elementwise_fma(pp, f32x2{blo(kv[i].z), bhi(kv[i].z)}, acc[2]);
          acc[3] = __builtin_elementwise_fma(pp, f32x2{blo(kv[i].w), bhi(kv[i].w)}, acc[3]);
          asm volatile("" : "+v"(acc[0]), "+v"(acc[1]), "+v"(acc[2]), "+v"(acc[3]));
        }
      });
      SCHED();
    });
    STAMP(4);
    f32x4 o4;
    o4.x = tr32(acc[0].x, acc[2].x) * scale; o4.y = tr32(acc[0].y, acc[2].y) * scale;
    o4.z = tr32(acc[1].x, acc[3].x) * scale; o4.w = tr32(acc[1].y, acc[3].y) * scale;
    *(LAS f32x4*)(L + L_O + (wave * 256 + c * 8 + half * 4) * 4) = o4;
    wg_barrier();
    STAMP(8);
    if (wave < 2) {
      const LAS f32x2* O2 = (const LAS f32x2*)(L + L_O) + tid;
      const f32x2 s = (O2[0] + O2[128]) + (O2[256] + O2[384]);
      bst128_sc1(rws, WS_O + ((bh * 8 + spl) * 128 + tid) * 16, u32x4{__float_as_uint(s.x), tag, __float_as_uint(s.y), tag});
    }
    STAMP(9);
    if (is_red) {
      const int b = g / 28, cg = g % 28;
      LAS f32x2* red = (LAS f32x2*)(L + L_RED);
      const int l8 = lane & 7;
      float x1a, x1b;
      {
        int vo[4]; bool ac[4] = {true, true, true, true}; u32x4 r[4];
#pragma unroll
        for (int i = 0; i < 4; ++i) vo[i] = WS_Y + ((b * 128 + 32 * wave + 8 * i + (lane >> 3)) * 224 + cg * 8 + l8) * 16;
        poll<4>(rws, vo, ac, tag, r);
        STAMP(10);
        float s0 = (__uint_as_float(r[0].x) + __uint_as_float(r[1].x)) + (__uint_as_float(r[2].x) + __uint_as_float(r[3].x));
        float s1 = (__uint_as_float(r[0].z) + __uint_as_float(r[1].z)) + (__uint_as_float(r[2].z) + __uint_as_float(r[3].z));
        s0 += dpp<ROR8>(s0); s1 += dpp<ROR8>(s1);
        s0 = tr16(s0, s0); s1 = tr16(s1, s1);
        s0 = tr32(s0, s0); s1 = tr32(s1, s1);
        if (lane < 8) red[wave * 8 + lane] = f32x2{s0, s1};
        wg_barrier();
        const f32x2 t = (red[l8] + red[8 + l8]) + (red[16 + l8] + red[24 + l8]);
        const u32 ga = ((const LAS u32*)(L + L_AUX))[l8], xx = ((const LAS u32*)(L + L_AUX + 64))[l8];
        x1a = rbf(blo(xx) + rbf(blo(ga) * rbf(t.x)));
        x1b = rbf(bhi(xx) + rbf(bhi(ga) * rbf(t.y)));
        if (wave == 0) {
          const u32 pk = pack2(x1a, x1b), nb = dppu<XOR1>(pk);
          if (lane < 8 && (lane & 1) == 0) bst128_sc1(rws, WS_X1 + g * 128 + (lane >> 1) * 16, u32x4{pk, tag, nb, tag});
        }
        STAMP(11);
      }
      {
        const bool a1 = lane < 48;
        const int p0 = 14 * wave + (lane >> 3), p1 = 14 * wave + (a1 ? 8 + (lane >> 3) : 0);
        int vo[2] = {WS_F + ((p0 * 2 + b) * 224 + cg * 8 + l8) * 16, WS_F + ((p1 * 2 + b) * 224 + cg * 8 + l8) * 16};
        bool ac[2] = {true, a1}; u32x4 r[2];
        poll<2>(rws, vo, ac, tag, r);
        STAMP(12);
        float s0 = __uint_as_float(r[0].x) + __uint_as_float(r[1].x);
        float s1 = __uint_as_float(r[0].z) + __uint_as_float(r[1].z);
        s0 += dpp<ROR8>(s0); s1 += dpp<ROR8>(s1);
        s0 = tr16(s0, s0); s1 = tr16(s1, s1);
        s0 = tr32(s0, s0); s1 = tr32(s1, s1);
        if (lane < 8) red[32 + wave * 8 + lane] = f32x2{s0, s1};
        wg_barrier();
        if (wave == 0 && lane < 8) {
          const f32x2 t = (red[32 + lane] + red[40 + lane]) + (red[48 + lane] + red[56 + lane]);
          const u32 gb = ((const LAS u32*)(L + L_AUX + 32))[lane];
          ((u32*)pout)[(b * D + cg * 16) / 2 + lane] = pack2(x1a + rbf(blo(gb) * rbf(t.x)), x1b + rbf(bhi(gb) * rbf(t.y)));
          if (g == 0 && lane == 0) __builtin_amdgcn_raw_buffer_store_b32(tag, rws, WS_EPOCH, 0, AUX_SC1);
        }
        STAMP(13);
      }
      return;
    }
    if (!is_ffn) return;
    load_wd(L, wave & 3, lane, wd);
    {
      const int lb = wave >> 1, lh = wave & 1;
      const bool act1 = lane < 48, lact = lh == 0 || act1;
      const int q1 = act1 ? 64 + lane : 0, lrec = lh == 0 ? lane : q1;
      const u32x2 shw = *(const LAS u32x2*)(L + L_LN + lb * 896 + lrec * 8), scw = *(const LAS u32x2*)(L + L_LN + 1792 + lb * 896 + lrec * 8);
      int vo[2] = {WS_X1 + (lb * 28 + (lane >> 2)) * 128 + (lane & 3) * 16, WS_X1 + (lb * 28 + (q1 >> 2)) * 128 + (q1 & 3) * 16};
      bool ac[2] = {true, act1}; u32x4 r[2];
      poll<2>(rws, vo, ac, tag, r);
      STAMP(11);
      const float xf[8] = {blo(r[0].x), bhi(r[0].x), blo(r[0].z), bhi(r[0].z), blo(r[1].x), bhi(r[1].x), blo(r[1].z), bhi(r[1].z)};
      float mean_b, rsd;
      ln_stats(xf, act1, mean_b, rsd);
      const u32 xw0 = lh ? r[1].x : r[0].x, xw1 = lh ? r[1].z : r[0].z;
      const float h0 = rbf(rbf(rbf(blo(xw0) - mean_b) * rsd) * blo(scw.x)) + blo(shw.x), h1 = rbf(rbf(rbf(bhi(xw0) - mean_b) * rsd) * bhi(scw.x)) + bhi(shw.x);
      const float h2 = rbf(rbf(rbf(blo(xw1) - mean_b) * rsd) * blo(scw.y)) + blo(shw.y), h3 = rbf(rbf(rbf(bhi(xw1) - mean_b) * rsd) * bhi(scw.y)) + bhi(shw.y);
      if (lact) *(LAS u32x2*)(L + L_H + lb * 1024 + lrec * 8) = u32x2{pack2(h0, h1), pack2(h2, h3)};
    }
  } else {
    const int a = wave - 4;
    __builtin_amdgcn_s_setprio(3);
    u32 ep = ald32_sc1(ws, WS_EPOCH);
    const bool act1 = lane < 48;
    const int qi = g - G_QP;
    const bool is_mu = is_qp && qi < 56;
    const int r0 = 4 * lane, r1 = act1 ? 256 + 4 * lane : 0;
    const int b = lane >> 5, rr = lane & 31;
    const bool actr = rr < 28;
    const int rec = 28 * a + (actr ? rr : 0);
    u32x2 xa0 = {0u, 0u}, xa1 = {0u, 0u}, xb0 = {0u, 0u}, xb1 = {0u, 0u}, xm = {0u, 0u};
    u32 c0 = 0u, c1 = 0u;
    if (is_mu || is_mod) {
      const int l56 = lane < 56 ? lane : 0;
      c0 = ald32(pc, (112 * a + 2 * l56) * 2); c1 = ald32(pc, (D + 112 * a + 2 * l56) * 2);
    }
    if (is_qp) {
      xa0 = ald64(px, r0 * 2); xa1 = ald64(px, r1 * 2); xb0 = ald64(px, (D + r0) * 2); xb1 = ald64(px, (D + r1) * 2);
      xm = ald64(px, (b * D + 4 * rec) * 2);
    }
    if (is_red) xm = ald64(px, ((g / 28) * D + (g % 28) * 16 + (lane & 3) * 4) * 2);
    if (a == 0 && lane == 0) { flags[0] = 0u; flags[1] = 0u; flags[3] = 0u; flags[4] = 0u; flags[5] = 0u; flags[6] = 0u; }
    SCHED();
    wg_barrier();
    const int ut = (qi & 7) * 7 + (qi >> 3);
    const int cg = is_mod ? 14 + g - G_MOD : qi;
    if (has_img) {
      if (!is_qp) {
        const unsigned long long t0 = __builtin_amdgcn_s_memrealtime();
        for (int i = 0; i < 4096 && __builtin_amdgcn_s_memrealtime() - t0 < IMGDELAY_; ++i) __builtin_amdgcn_s_sleep(4);
      }
      if (is_mu) {
        const char* src = (const char*)pwmod + (size_t)(128 * a + (lane >> 1)) * (NM * 2) + ut * 32 + (lane & 1) * 16;
#pragma unroll
        for (int i = 0; i < 4; ++i)
          if (4 * a + i < 14) dma16(src + (size_t)i * (32 * NM * 2), lds0 + L_W2 + (4 * a + i) * 1024);
      }
      {
        const int ch = lane & 7, row = 112 * a + (lane >> 3);
        const char* src;
        int step;
        if (is_mod) { src = (const char*)pwmod + (size_t)row * (NM * 2) + cg * 128 + ch * 16; step = 8 * NM * 2; }
        else if (is_qp) { src = (const char*)pwq + (size_t)row * (NH * DH * 2) + cg * 128 + ch * 16; step = 8 * NH * DH * 2; }
        else { src = (const char*)((ch & 1) ? pwu : pwg) + (size_t)row * (FF * 2) + fcg * 64 + (ch >> 1) * 16; step = 8 * FF * 2; }
#pragma unroll
        for (int i = 0; i < 14; ++i) dma16(src + (size_t)i * step, lds0 + L_W + (112 * a + 8 * i) * 128);
      }
      if (is_ffn) {
#pragma unroll
        for (int i = 0; i < 7; ++i) dma16((const char*)pwd + (size_t)fcg * 32 * (D * 2) + (a * 7 + i) * 1024 + lane * 16, lds0 + L_W2 + (a * 7 + i) * 1024);
      }
    }
    asm volatile("s_waitcnt vmcnt(16)" ::: "memory");
    if (!is_mu) asm volatile("s_waitcnt vmcnt(14)" ::: "memory");
    if (!has_img) asm volatile("s_waitcnt vmcnt(0)" ::: "memory");
    SCHED();
    asm volatile("" : "+v"(ep), "+v"(c0), "+v"(c1), "+v"(xa0), "+v"(xa1), "+v"(xb0), "+v"(xb1), "+v"(xm));
    SCHED();
    tag = __builtin_amdgcn_readfirstlane(ep) + 1u;
    if (has_img) {
      if ((is_mu || is_mod) && lane < 56) {
        const float f0 = blo(c0), f1 = bhi(c0), f2 = blo(c1), f3 = bhi(c1);
        *(LAS u32*)(L + L_H + (112 * a + 2 * lane) * 2) = pack2(f0 / (1.f + expf(-f0)), f1 / (1.f + expf(-f1)));
        *(LAS u32*)(L + L_H + 1024 + (112 * a + 2 * lane) * 2) = pack2(f2 / (1.f + expf(-f2)), f3 / (1.f + expf(-f3)));
      }
      float mean_b = 0.f, rsd_b = 0.f;
      if (is_qp) {
        float mean0, sd0, mean1, sd1;
        { const float xf[8] = {blo(xa0.x), bhi(xa0.x), blo(xa0.y), bhi(xa0.y), blo(xa1.x), bhi(xa1.x), blo(xa1.y), bhi(xa1.y)}; ln_stats(xf, act1, mean0, sd0); }
        { const float xf[8] = {blo(xb0.x), bhi(xb0.x), blo(xb0.y), bhi(xb0.y), blo(xb1.x), bhi(xb1.x), blo(xb1.y), bhi(xb1.y)}; ln_stats(xf, act1, mean1, sd1); }
        mean_b = b ? mean1 : mean0; rsd_b = b ? sd1 : sd0;
      }
      if (is_mu) {
        CBAR(); WAIT_VM(14); CBAR();
        aux_sync(L, 6, lane);
        STAMP(1);
        if (a == 0) {
          load_afrags<32>(L + L_W2, lane, af);
          const f32x4 d = gemv_tile(L + L_H, lane, af);
          if ((lane & 15) < 2) bst128_sc1(rws, WS_MU + ut * 128 + (lane & 1) * 64 + (lane >> 4) * 16, u32x4{pack2(d.x, d.y), tag, pack2(d.z, d.w), tag});
          STAMP(2);
        }
      }
      CBAR(); WAIT_VM(0); CBAR();
      aux_sync(L, 3, lane);
      STAMP(3);
      if (!is_qp && a == 0 && lane == 0) flags[5] = 1u;
      load_afrags<128>(L + L_W + 32 * a, lane, af);
      if (!is_ffn) {
        if (is_qp) {
          const int t0 = rec >> 2;
          int po[2] = {WS_MU + t0 * 128 + b * 64 + (rec & 3) * 16, WS_MU + (28 + t0) * 128 + b * 64 + (rec & 3) * 16};
          bool ac[2] = {actr, actr};
          u32x4 r[2];
          poll<2>(rws, po, ac, tag, r);
          STAMP(4);
          if (actr) {
            u32x2 hv;
            hv.x = pack2(ln_one(blo(xm.x), mean_b, rsd_b, blo(r[0].x), blo(r[1].x)), ln_one(bhi(xm.x), mean_b, rsd_b, bhi(r[0].x), bhi(r[1].x)));
            hv.y = pack2(ln_one(blo(xm.y), mean_b, rsd_b, blo(r[0].z), blo(r[1].z)), ln_one(bhi(xm.y), mean_b, rsd_b, bhi(r[0].z), bhi(r[1].z)));
            *(LAS u32x2*)(L + L_H2 + b * 1024 + rec * 8) = hv;
          }
          aux_sync(L, 4, lane);
        }
        const f32x4 d = gemv_tile(L + (is_qp ? L_H2 : L_H), lane, af);
        if ((lane & 15) < 2) {
          const int ro = (is_mod ? WS_MOD + (lane & 1) * (672 * 16) : WS_Q + (lane & 1) * (1024 * 16)) + (cg * 16 + a * 4 + (lane >> 4)) * 16;
          bst128_sc1(rws, ro, u32x4{pack2(d.x, d.y), tag, pack2(d.z, d.w), tag});
        }
        STAMP(5);
      }
    } else {
      if (a == 0) {
        const unsigned long long t0 = __builtin_amdgcn_s_memrealtime();
        for (int i = 0; i < 4096 && __builtin_amdgcn_s_memrealtime() - t0 < QSLEEP_; ++i) __builtin_amdgcn_s_sleep(4);
      }
    }
    if (a == 0) {
      int vo[1] = {WS_Q + (bat * 1024 + head * 64 + lane) * 16};
      bool ac[1] = {true};
      u32x4 r[1];
      poll<1>(rws, vo, ac, tag, r);
      u32x2 qq = {r[0].x, r[0].z};
      *(LAS u32x2*)(L + L_Q + lane * 8) = qq;
      if (lane == 0) flags[2] = tag;
      LDS_FENCE();
      if (lane == 0) { flags[0] = 1u; flags[5] = 1u; }
      STAMP(6);
    } else {
      lds_wait(flags + 0);
    }
    u32 wo[32];
    {
      const rsrc_t rwo = mk_rsrc(pwo, NH * DH * D * 2);
      const int l56 = lane < 56 ? lane : 0;
      const int wbase = (head * DH + spl * 32) * (D * 2);
#pragma unroll
      for (int i = 0; i < 32; ++i) {
        if (i >= 8) WAIT_VM(7);
        wo[i] = bld32<AUX_NT>(rwo, (112 * a + 2 * l56) * 2, wbase + i * (D * 2));
        SCHED();
      }
#pragma unroll
      for (int p = 0; p < 16; ++p) {
        const u32 lo = perm_lo(wo[2 * p], wo[2 * p + 1]), hi = perm_hi(wo[2 * p], wo[2 * p + 1]);
        wo[2 * p] = lo; wo[2 * p + 1] = hi;
        PIN2(wo[2 * p], wo[2 * p + 1]);
      }
    }
    if (is_red && a == 1) {
      const int rb = g / 28, rcg = g % 28;
      int vo[1] = {WS_MOD + (rb * 672 + ((lane & 4) ? 560 : 224) + rcg * 4 + (lane & 3)) * 16};
      bool ac[1] = {lane < 8};
      u32x4 r[1];
      poll<1, 8>(rws, vo, ac, tag, r);
      if (lane < 8) *(LAS u32x2*)(L + L_AUX + lane * 8) = u32x2{r[0].x, r[0].z};
      if (lane < 4) *(LAS u32x2*)(L + L_AUX + 64 + lane * 8) = xm;
    }
    if (is_ffn) {
      const int lb = a >> 1, lh = a & 1;
      const bool lact = lh == 0 || act1;
      const int lrec = lh == 0 ? lane : (act1 ? 64 + lane : 0);
      int vo[2] = {WS_MOD + (lb * 672 + 336 + lrec) * 16, WS_MOD + (lb * 672 + 448 + lrec) * 16};
      bool ac[2] = {lact, lact};
      u32x4 md[2];
      poll<2, 8>(rws, vo, ac, tag, md);
      if (lact) {
        *(LAS u32x2*)(L + L_LN + lb * 896 + lrec * 8) = u32x2{md[0].x, md[0].z};
        *(LAS u32x2*)(L + L_LN + 1792 + lb * 896 + lrec * 8) = u32x2{pack2(1.f + blo(md[1].x), 1.f + bhi(md[1].x)), pack2(1.f + blo(md[1].z), 1.f + bhi(md[1].z))};
      }
      load_wd(L, a, lane, wd);
    }
    STAMP(7);
    wg_barrier();
    STAMP(8);
    {
      float wgt;
      {
        int vo[1] = {WS_ST + (bh * 8 + (lane >> 3)) * 128};
        bool ac[1] = {true};
        u32x4 r[1];
        poll<1>(rws, vo, ac, tag, r);
        const float ms = __uint_as_float(r[0].x), ls = __uint_as_float(r[0].z);
        const float M = max32(max16(fmaxf(ms, dpp<ROR8>(ms))));
        const float e = __expf(ms - M);
        float t = e * ls;
        t += dpp<ROR8>(t); t = tr16(t, t); t = tr32(t, t);
        wgt = e / t;
        asm volatile("" : "+v"(wgt));
      }
      const int ob = WS_O + ((bh * 8 + (lane >> 3)) * 128 + spl * 16 + (lane & 7)) * 16;
      int vo[2] = {ob, ob + 128};
      bool ac[2] = {true, true};
      u32x4 r[2];
      poll<2>(rws, vo, ac, tag, r);
      STAMP(9);
      float s0 = __uint_as_float(r[0].x) * wgt, s1 = __uint_as_float(r[0].z) * wgt, s2 = __uint_as_float(r[1].x) * wgt, s3 = __uint_as_float(r[1].z) * wgt;
      s0 += dpp<ROR8>(s0); s1 += dpp<ROR8>(s1); s2 += dpp<ROR8>(s2); s3 += dpp<ROR8>(s3);
      s0 = tr16(s0, s2); s1 = tr16(s1, s3);
      s0 = tr32(s0, s0); s1 = tr32(s1, s1);
      const u32 op = pack2(s0, s1);
      float y0 = 0.f, y1 = 0.f, y2 = 0.f, y3 = 0.f;
#pragma unroll
      for (int p = 0; p < 8; ++p) {
        const u32 oa = (u32)__builtin_amdgcn_readlane((int)op, p), oc = (u32)__builtin_amdgcn_readlane((int)op, 16 + p);
        y0 = dot2(wo[2 * p], oa, y0); y1 = dot2(wo[2 * p + 1], oa, y1);
        y2 = dot2(wo[16 + 2 * p], oc, y2); y3 = dot2(wo[16 + 2 * p + 1], oc, y3);
      }
      if (lane < 56) bst128_sc1(rws, WS_Y + ((bat * 128 + head * 8 + spl) * 224 + 56 * a + lane) * 16, u32x4{__float_as_uint(y0 + y2), tag, __float_as_uint(y1 + y3), tag});
      STAMP(10);
    }
    if (!is_ffn) return;
  }

  {
    const int db = wave >> 2, dq = wave & 3;
    wg_barrier();
    STAMP(12);
    if (wave >= 4) {
      const f32x4 d = gemv_tile(L + L_H, lane, af);
      float ak[4];
#pragma unroll
      for (int i = 0; i < 4; ++i) {
        auto s = __builtin_amdgcn_permlane32_swap(__float_as_uint(d[i]), __float_as_uint(d[i]), false, false);
        const float gg = rbf(__uint_as_float(s[0])), uu = rbf(__uint_as_float(s[1]));
        const float z = 0.7978845608028654f * (gg + 0.044715f * gg * gg * gg);
        const float th = 1.f - 2.f * __builtin_amdgcn_rcpf(__expf(2.f * z) + 1.f);
        ak[i] = rbf(0.5f * gg * (1.f + th)) * uu;
      }
      if (lane < 32 && (lane & 15) < 2)
        *(LAS u32x2*)(L + L_ACT + (lane & 1) * 64 + (8 * (wave - 4) + 4 * (lane >> 4)) * 2) = u32x2{pack2(ak[0], ak[1]), pack2(ak[2], ak[3])};
    }
    wg_barrier();
    STAMP(13);
    const LAS u32x4* ap = (const LAS u32x4*)(L + L_ACT + db * 64);
    const u32x4 a0 = ap[0], a1 = ap[1], a2 = ap[2], a3 = ap[3];
    const u32 av[16] = {a0.x, a0.y, a0.z, a0.w, a1.x, a1.y, a1.z, a1.w, a2.x, a2.y, a2.z, a2.w, a3.x, a3.y, a3.z, a3.w};
    float y0 = 0.f, y1 = 0.f, y2 = 0.f, y3 = 0.f;
#pragma unroll
    for (int p = 0; p < 8; ++p) {
      y0 = dot2(wd[2 * p], av[p], y0); y1 = dot2(wd[2 * p + 1], av[p], y1);
      y2 = dot2(wd[16 + 2 * p], av[8 + p], y2); y3 = dot2(wd[16 + 2 * p + 1], av[8 + p], y3);
    }
    if (lane < 56) bst128_sc1(rws, WS_F + ((fi * 2 + db) * 224 + 56 * dq + lane) * 16, u32x4{__float_as_uint(y0 + y2), tag, __float_as_uint(y1 + y3), tag});
    STAMP(14);
  }
}

extern "C" void kernel_launch(void* const* d_in, const int* in_sizes, int n_in,
                              void* d_out, int out_size, void* d_ws, size_t ws_size,
                              hipStream_t stream) {
  fused_decode_kernel<<<NWG, NTHR, 0, stream>>>(d_in[0], d_in[1], d_in[2], d_in[3], d_in[4], d_in[6], d_in[7], d_in[8],
                                                d_in[9], d_in[10], d_out, d_ws);
}
```

```cpp
#include <hip/hip_runtime.h>
#include <stdint.h>

typedef unsigned u32;
typedef unsigned short u16;
typedef u32 u32x4 __attribute__((ext_vector_type(4)));
typedef u32 u32x2 __attribute__((ext_vector_type(2)));
typedef float f32x4 __attribute__((ext_vector_type(4)));
typedef float f32x2 __attribute__((ext_vector_type(2)));
typedef __bf16 bf16x2 __attribute__((ext_vector_type(2)));
typedef short s16x4 __attribute__((ext_vector_type(4)));
typedef short s16x8 __attribute__((ext_vector_type(8)));
typedef __bf16 bf16x8 __attribute__((ext_vector_type(8)));
typedef __amdgpu_buffer_rsrc_t rsrc_t;
#define LAS __attribute__((address_space(3)))

namespace {

constexpr int D = 448, NH = 16, SK = 4096, DH = 256, FF = 1792, NM = 6 * D;
constexpr int NWG = 256, NTHR = 512;
#define CLSEXPR (spl < 3 ? 1 : 0)
#define RING_ 56
#define WA_ 8
#define WB_ 7
#define NSA_ 72
#define NSF_ 60
#define NSM_ 64
#define IMGDELAY_ 400
#define QSLEEP_ 400
constexpr int NS = 72;
constexpr int RING = RING_;
constexpr int WA = WA_, WB = WB_;
constexpr int NSA = NSA_, NSF = NSF_, NSM = NSM_, NSQ = (512 - 3 * NSA - 2 * NSF - NSM) / 2;
static_assert(3 * NSA + 2 * NSF + NSM + 2 * NSQ == 512 && NSA <= NS && NSF <= NS && NSM <= NS && NSQ <= NS, "");
static_assert(NS - NSQ <= 32 && NS - NSF <= 32 && NS - NSM <= 32 && NSQ >= 32 && NSF >= 32 && NSM >= 32, "");
constexpr int G_RED = 0, N_RED = 56;
constexpr int G_FFN = 96, N_FFN = 56;
constexpr int G_MOD = 160, N_MOD = 28;
constexpr int G_QP = 192, N_QP = 64;

constexpr int WS_EPOCH = 0;
constexpr int WS_MOD = 4096;
constexpr int WS_Q = 32768;
constexpr int WS_ST = WS_Q + 2 * 1024 * 16;
constexpr int WS_X1 = WS_ST + 256 * 128;
constexpr int WS_O = WS_X1 + 8192;
constexpr int WS_Y = WS_O + 256 * 128 * 16;
constexpr int WS_F = WS_Y + 256 * 224 * 16;
constexpr int WS_END = WS_F + N_FFN * 2 * 224 * 16;
constexpr int WS_STAMP = 8 << 20;

constexpr int AUX_NT = 2, AUX_SC1 = 16;
constexpr u32 SPIN_CAP = 1u << 22;

constexpr int L_FLAG = 0;
constexpr int L_ML = 64;
constexpr int L_AUX = 128;
constexpr int L_Q = 256;
constexpr int L_P = L_Q + 512;
constexpr int L_O = L_P + 8 * NS * 4;
constexpr int L_H = L_O + 4096;
constexpr int L_H2 = L_H + 2048;
constexpr int L_RED = L_H2 + 2048;
constexpr int L_ACT = L_RED + 1024;
constexpr int L_LN = L_ACT + 256;
constexpr int L_W = L_LN + 3584;
constexpr int L_W2 = L_W + 448 * 128;
constexpr int L_END = L_W2 + 32 * 896;
static_assert(L_END <= 160 * 1024 && (L_O % 16) == 0 && (L_W % 16) == 0, "");

__device__ __forceinline__ rsrc_t mk_rsrc(const void* p, u32 bytes) {
  return __builtin_amdgcn_make_buffer_rsrc((void*)p, 0, (int)bytes, 0x00020000);
}
template <int AUX> __device__ __forceinline__ u32x4 bld128(rsrc_t r, int voff, int soff) {
  return __builtin_amdgcn_raw_buffer_load_b128(r, voff, soff, AUX);
}
template <int AUX> __device__ __forceinline__ u32x2 bld64(rsrc_t r, int voff, int soff) {
  return __builtin_amdgcn_raw_buffer_load_b64(r, voff, soff, AUX);
}
template <int AUX> __device__ __forceinline__ u32 bld32(rsrc_t r, int voff, int soff) {
  return __builtin_amdgcn_raw_buffer_load_b32(r, voff, soff, AUX);
}
__device__ __forceinline__ void bst128_sc1(rsrc_t r, int voff, u32x4 v) {
  __builtin_amdgcn_raw_buffer_store_b128(v, r, voff, 0, AUX_SC1);
}
__device__ __forceinline__ float dot2(u32 a, u32 b, float c) {
  return __builtin_amdgcn_fdot2_f32_bf16(__builtin_bit_cast(bf16x2, a), __builtin_bit_cast(bf16x2, b), c, false);
}
__device__ __forceinline__ u32 pack2(float lo, float hi) {
  bf16x2 v = {(__bf16)lo, (__bf16)hi};
  return __builtin_bit_cast(u32, v);
}
__device__ __forceinline__ float rbf(float f) { return (float)(__bf16)f; }
__device__ __forceinline__ float blo(u32 v) { return __uint_as_float(v << 16); }
__device__ __forceinline__ float bhi(u32 v) { return __uint_as_float(v & 0xffff0000u); }
__device__ __forceinline__ u32 perm_lo(u32 a, u32 b) { return __builtin_amdgcn_perm(b, a, 0x05040100u); }
__device__ __forceinline__ u32 perm_hi(u32 a, u32 b) { return __builtin_amdgcn_perm(b, a, 0x07060302u); }
template <int CTRL> __device__ __forceinline__ float dpp(float x) {
  return __builtin_bit_cast(float, __builtin_amdgcn_mov_dpp(__builtin_bit_cast(int, x), CTRL, 0xf, 0xf, true));
}
template <int CTRL> __device__ __forceinline__ u32 dppu(u32 x) {
  return (u32)__builtin_amdgcn_mov_dpp((int)x, CTRL, 0xf, 0xf, true);
}
constexpr int XOR1 = 0xB1, XOR2 = 0x4E, XOR7 = 0x141, ROR8 = 0x128, ROR4 = 0x124, ROR2 = 0x122, ROR1 = 0x121;
__device__ __forceinline__ float tr32(float a, float b) {
  auto s = __builtin_amdgcn_permlane32_swap(__float_as_uint(a), __float_as_uint(b), false, false);
  return __uint_as_float(s[0]) + __uint_as_float(s[1]);
}
__device__ __forceinline__ float tr16(float a, float b) {
  auto s = __builtin_amdgcn_permlane16_swap(__float_as_uint(a), __float_as_uint(b), false, false);
  return __uint_as_float(s[0]) + __uint_as_float(s[1]);
}
__device__ __forceinline__ float max16(float x) {
  auto s = __builtin_amdgcn_permlane16_swap(__float_as_uint(x), __float_as_uint(x), false, false);
  return fmaxf(__uint_as_float(s[0]), __uint_as_float(s[1]));
}
__device__ __forceinline__ float max32(float x) {
  auto s = __builtin_amdgcn_permlane32_swap(__float_as_uint(x), __float_as_uint(x), false, false);
  return fmaxf(__uint_as_float(s[0]), __uint_as_float(s[1]));
}
__device__ __forceinline__ float row_sum(float x) {
  x += dpp<ROR8>(x); x += dpp<ROR4>(x); x += dpp<ROR2>(x); x += dpp<ROR1>(x);
  return x;
}
__device__ __forceinline__ float wave_sum(float x) { x = row_sum(x); x = tr16(x, x); return tr32(x, x); }
__device__ __forceinline__ float wave_max(float x) {
  x = fmaxf(x, dpp<ROR8>(x)); x = fmaxf(x, dpp<ROR4>(x)); x = fmaxf(x, dpp<ROR2>(x)); x = fmaxf(x, dpp<ROR1>(x));
  return max32(max16(x));
}
constexpr int vmcnt_imm(int n) { return (n & 15) | ((n >> 4) << 14) | (7 << 4) | (15 << 8); }
#define WAIT_VM(n) __builtin_amdgcn_s_waitcnt(vmcnt_imm(n))
#define WAIT_LGKM0() __builtin_amdgcn_s_waitcnt(0xC07F)
#define SCHED() __builtin_amdgcn_sched_barrier(0)
#define CBAR() asm volatile("" ::: "memory")
#define LDS_FENCE() do { CBAR(); WAIT_LGKM0(); CBAR(); } while (0)
#define PIN2(a, b) asm volatile("" : "+v"(a), "+v"(b))
__device__ __forceinline__ void wg_barrier() { LDS_FENCE(); __builtin_amdgcn_s_barrier(); CBAR(); }

#define STAMP(i) do { } while (0)

template <int N, int SLEEP = 0>
__device__ __forceinline__ void poll(rsrc_t ws, const int (&voff)[N], const bool (&act)[N], u32 tag, u32x4 (&r)[N]) {
  bool ok[N];
#pragma unroll
  for (int i = 0; i < N; ++i) { ok[i] = !act[i]; r[i] = u32x4{0u, 0u, 0u, 0u}; }
  for (u32 spin = 0; spin < SPIN_CAP; ++spin) {
    CBAR();
#pragma unroll
    for (int i = 0; i < N; ++i) if (!ok[i]) r[i] = bld128<AUX_SC1>(ws, voff[i], 0);
    bool all = true;
#pragma unroll
    for (int i = 0; i < N; ++i) { ok[i] = ok[i] || (r[i].y == tag && r[i].w == tag); all = all && ok[i]; }
    if (__all(all)) break;
    if (SLEEP) __builtin_amdgcn_s_sleep(SLEEP);
  }
  CBAR();
}
__device__ __forceinline__ void lds_wait(volatile LAS u32* f, u32 want = 1u) {
  for (u32 spin = 0; spin < SPIN_CAP; ++spin) {
    if (*f >= want) break;
    __builtin_amdgcn_s_sleep(1);
  }
  CBAR();
}
__device__ __forceinline__ u32 lds_inc(LAS unsigned char* L, int idx) {
  return __hip_atomic_fetch_add((LAS u32*)(L + L_FLAG) + idx, 1u, __ATOMIC_RELAXED, __HIP_MEMORY_SCOPE_WORKGROUP);
}
__device__ __forceinline__ void aux_sync(LAS unsigned char* L, int idx, int lane) {
  LDS_FENCE();
  if (lane == 0) lds_inc(L, idx);
  volatile LAS u32* f = (volatile LAS u32*)(L + L_FLAG) + idx;
  for (u32 spin = 0; spin < SPIN_CAP; ++spin) if (*f >= 4u) break;
  CBAR();
}

__device__ __forceinline__ void ln_stats(const float (&x)[8], bool act1, float& mean_b, float& rsd) {
  float s = (x[0] + x[1]) + (x[2] + x[3]);
  float s1 = (x[4] + x[5]) + (x[6] + x[7]);
  s += act1 ? s1 : 0.f;
  const float mean = wave_sum(s) * (1.0f / D);
  float v = 0.f, v1 = 0.f;
#pragma unroll
  for (int i = 0; i < 4; ++i) { float d = x[i] - mean; v = fmaf(d, d, v); float e = x[4 + i] - mean; v1 = fmaf(e, e, v1); }
  v += act1 ? v1 : 0.f;
  const float var = wave_sum(v) * (1.0f / D);
  mean_b = rbf(mean);
  rsd = __builtin_amdgcn_rcpf(rbf(__builtin_amdgcn_sqrtf(rbf(rbf(var) + 1e-5f))));
}
__device__ __forceinline__ float ln_one(float x, float mean_b, float rsd, float sh, float sc) {
  const float n = rbf(rbf(x - mean_b) * rsd);
  return rbf(n * rbf(1.f + sc)) + sh;
}

template <int I> struct ic { static constexpr int value = I; };
template <int B, int E, class F> __device__ __forceinline__ void static_for(F&& f) {
  if constexpr (B < E) { f(ic<B>{}); static_for<B + 1, E>(f); }
}
constexpr int cons_upto(int t) {
  if (t < 0) return 0;
  if (RING + t >= 2 * NS) return 2 * NS;
  int a = 4 * (t + 1), b = RING + t + 1 - WA;
  return a < b ? a : b;
}
#define PACE() do { WAIT_VM(WA - 1); if (WB < WA && cls != 0) WAIT_VM(WB - 1); } while (0)

__device__ __forceinline__ void dma16(const void* src, u32 lds_dst) {
  unsigned keep;
  asm volatile("s_mov_b32 %0, m0\n\ts_mov_b32 m0, %2\n\ts_nop 0\n\tglobal_load_lds_dwordx4 %1, off nt\n\ts_mov_b32 m0, %0"
               : "=&s"(keep) : "v"(src), "s"(lds_dst) : "memory");
}
__device__ __forceinline__ u32 ald32(const void* base, int voff) {
  u32 r; asm volatile("global_load_dword %0, %1, %2 nt" : "=v"(r) : "v"(voff), "s"(base) : "memory"); return r;
}
__device__ __forceinline__ u32 ald32_sc1(const void* base, int voff) {
  u32 r; asm volatile("global_load_dword %0, %1, %2 sc1" : "=v"(r) : "v"(voff), "s"(base) : "memory"); return r;
}
__device__ __forceinline__ u32x2 ald64(const void* base, int voff) {
  u32x2 r; asm volatile("global_load_dwordx2 %0, %1, %2 nt" : "=v"(r) : "v"(voff), "s"(base) : "memory"); return r;
}
template <int PITCH>
__device__ __forceinline__ void load_afrags(LAS unsigned char* img, int lane, s16x8 (&af)[14]) {
  LAS unsigned char* p = img + (8 * (lane >> 4) + ((lane & 15) >> 2)) * PITCH + (lane & 3) * 8;
#pragma unroll
  for (int s = 0; s < 14; ++s) {
    const s16x4 lo = __builtin_amdgcn_ds_read_tr16_b64_v4i16((LAS s16x4*)(p + s * 32 * PITCH));
    const s16x4 hi = __builtin_amdgcn_ds_read_tr16_b64_v4i16((LAS s16x4*)(p + s * 32 * PITCH + 4 * PITCH));
    af[s] = __builtin_shufflevector(lo, hi, 0, 1, 2, 3, 4, 5, 6, 7);
    asm volatile("" : "+v"(af[s]));
  }
}
__device__ __forceinline__ f32x4 gemv_tile(LAS unsigned char* h, int lane, const s16x8 (&af)[14]) {
  const LAS u32x4* hp = (const LAS u32x4*)(h + (lane & 1) * 1024 + (lane >> 4) * 16);
  u32x4 bf[14];
#pragma unroll
  for (int s = 0; s < 14; ++s) bf[s] = hp[s * 4];
  f32x4 acc0 = {0.f, 0.f, 0.f, 0.f}, acc1 = {0.f, 0.f, 0.f, 0.f};
#pragma unroll
  for (int s = 0; s < 14; s += 2) {
    acc0 = __builtin_amdgcn_mfma_f32_16x16x32_bf16(__builtin_bit_cast(bf16x8, af[s]), __builtin_bit_cast(bf16x8, bf[s]), acc0, 0, 0, 0);
    acc1 = __builtin_amdgcn_mfma_f32_16x16x32_bf16(__builtin_bit_cast(bf16x8, af[s + 1]), __builtin_bit_cast(bf16x8, bf[s + 1]), acc1, 0, 0, 0);
  }
  return acc0 + acc1;
}
__device__ __forceinline__ void load_wd(LAS unsigned char* L, int dq, int lane, u32 (&wd)[32]) {
  const int l56 = lane < 56 ? lane : 0;
  const LAS u32* wp = (const LAS u32*)(L + L_W2 + (112 * dq + 2 * l56) * 2);
#pragma unroll
  for (int p = 0; p < 16; ++p) {
    const u32 ra = wp[(2 * p) * 224], rb = wp[(2 * p + 1) * 224];
    wd[2 * p] = perm_lo(ra, rb); wd[2 * p + 1] = perm_hi(ra, rb);
    PIN2(wd[2 * p], wd[2 * p + 1]);
  }
}

}

__global__ void __launch_bounds__(NTHR, 2)
fused_decode_kernel(const void* pK, const void* pV, void* ws, const void* px, const void* pc, const void* pwmod,
                    const void* pwq, const void* pwo, const void* pwg, const void* pwu, const void* pwd, void* pout) {
  asm volatile("" : "+s"(pK), "+s"(pV), "+s"(ws), "+s"(px), "+s"(pc), "+s"(pwmod), "+s"(pwq), "+s"(pwo), "+s"(pwg), "+s"(pwu), "+s"(pwd), "+s"(pout));
  __shared__ __attribute__((aligned(16))) unsigned char lds_raw[L_END];
  LAS unsigned char* L = (LAS unsigned char*)lds_raw;
  const int tid = threadIdx.x, lane = tid & 63;
  const int wave = __builtin_amdgcn_readfirstlane(tid >> 6);
  const int g = blockIdx.x;
  const int head = 2 * (g & 7) + ((g >> 3) & 1), bat = (g >> 4) & 1, spl = g >> 5;
  const int bh = bat * NH + head;
  const bool is_red = g < G_RED + N_RED, is_ffn = g >= G_FFN && g < G_FFN + N_FFN;
  const bool is_mod = g >= G_MOD && g < G_MOD + N_MOD, is_qp = g >= G_QP;
  const bool has_img = is_mod || is_qp || is_ffn;
  const bool is_mu = is_mod && g < G_MOD + 14;
  const int fi = g - G_FFN;
  const int fcg = fi < 48 ? (fi >> 4) * 16 + (fi & 7) * 2 + ((fi >> 3) & 1) : fi;
  const rsrc_t rws = mk_rsrc(ws, WS_END);
  volatile LAS u32* flags = (volatile LAS u32*)(L + L_FLAG);
  const u32 lds0 = (u32)(uintptr_t)L;
  STAMP(0);
  u32 tag;
  u32 wd[32];
  s16x8 af[14];

  if (wave < 4) {
    const int ns = spl < 3 ? NSA : spl < 5 ? NSF : spl < 6 ? NSM : NSQ;
    const int s0 = spl < 3 ? spl * NSA : spl < 5 ? 3 * NSA + (spl - 3) * NSF : spl < 6 ? 3 * NSA + 2 * NSF : 3 * NSA + 2 * NSF + NSM + (spl - 6) * NSQ;
    const int cls = CLSEXPR;
    const size_t chunk = ((size_t)bh * SK) * (DH * 2) + (size_t)s0 * 4096;
    const rsrc_t rK = mk_rsrc((const char*)pK + chunk, ns * 4096);
    const rsrc_t rV = mk_rsrc((const char*)pV + chunk, ns * 4096);
    const int voff = wave * 1024 + lane * 16;
    const int shift = NS - ns;
    const int voffv = voff - shift * 4096;
    const int half = lane >> 5, c = lane & 31;
    LAS float* P = (LAS float*)(L + L_P) + (wave * 2 + half) * NS;
    P[c] = 0.f;
    u32x4 kv[2 * NS];
    wg_barrier();
    if (is_qp || is_mu) lds_wait(flags + 5);
    SCHED();
    STAMP(5);
#pragma unroll
    for (int i = 0; i < RING; ++i) {
      if (i >= WB) PACE();
      kv[i] = bld128<AUX_NT>(rK, voff + i * 4096, 0);
      SCHED();
    }
    STAMP(1);
    lds_wait(flags + 0);
    STAMP(2);
    tag = flags[2];
    const u32x4 q = *(const LAS u32x4*)(L + L_Q + c * 16);
    float lvl[5];
    float sc[3];
    f32x2 acc[4];
#pragma unroll
    for (int k = 0; k < 4; ++k) acc[k] = f32x2{0.f, 0.f};
    float m_w = 0.f, scale = 0.f;
    f32x4 pv = {0.f, 0.f, 0.f, 0.f};
    static_for<0, 2 * NS - RING + 1>([&](auto tc) {
      constexpr int t = decltype(tc)::value;
      constexpr int e = RING + t;
      if constexpr (e < 2 * NS) {
        PACE();
        if constexpr (e < NS) kv[e] = bld128<AUX_NT>(rK, voff + e * 4096, 0);
        else kv[e] = bld128<AUX_NT>(rV, voffv + (e - NS) * 4096, 0);
        SCHED();
      }
      if constexpr (e == 2 * NS - 6) {
        lds_wait(flags + 1, 4u);
        const LAS f32x2* ml = (const LAS f32x2*)(L + L_ML);
        const float M = fmaxf(fmaxf(ml[0].x, ml[1].x), fmaxf(ml[2].x, ml[3].x));
        scale = __expf(m_w - M);
        asm volatile("" : "+v"(scale));
      }
      static_for<cons_upto(t - 1), cons_upto(t)>([&](auto icn) {
        constexpr int i = decltype(icn)::value;
        if constexpr (i < NS) {
          float d = dot2(kv[i].x, q.x, 0.f);
          d = dot2(kv[i].y, q.y, d); d = dot2(kv[i].z, q.z, d); d = dot2(kv[i].w, q.w, d);
          constexpr int j = i & 31;
          if constexpr ((j & 1) == 0) lvl[0] = d;
          else {
            float x = tr16(lvl[0], d);
            if constexpr ((j & 2) == 0) lvl[1] = x;
            else {
              float a = lvl[1] + dpp<ROR8>(lvl[1]), b = x + dpp<ROR8>(x);
              x = (lane & 8) ? b : a;
              if constexpr ((j & 4) == 0) lvl[2] = x;
              else {
                a = lvl[2] + dpp<XOR7>(lvl[2]); b = x + dpp<XOR7>(x);
                x = (lane & 4) ? b : a;
                if constexpr (i == 71) { x += dpp<XOR2>(x); sc[2] = x + dpp<XOR1>(x); }
                else if constexpr ((j & 8) == 0) lvl[3] = x;
                else {
                  a = lvl[3] + dpp<XOR2>(lvl[3]); b = x + dpp<XOR2>(x);
                  x = (lane & 2) ? b : a;
                  if constexpr ((j & 16) == 0) lvl[4] = x;
                  else {
                    a = lvl[4] + dpp<XOR1>(lvl[4]); b = x + dpp<XOR1>(x);
                    sc[i >> 5] = (lane & 1) ? b : a;
                  }
                }
              }
            }
          }
          if constexpr (i == NS - 1) {
            const int sl = ((lane >> 4) & 1) | ((lane >> 3) & 1) << 1 | ((lane >> 2) & 1) << 2 | ((lane >> 1) & 1) << 3 | (lane & 1) << 4;
            const bool v1 = 32 + sl < ns, v2 = sl < 8 && 64 + sl < ns;
            const float t0 = rbf(sc[0]) * 0.0625f;
            const float t1 = v1 ? rbf(sc[1]) * 0.0625f : -INFINITY, t2 = v2 ? rbf(sc[2]) * 0.0625f : -INFINITY;
            m_w = wave_max(fmaxf(fmaxf(t0, t1), t2));
            const float p0 = __expf(t0 - m_w), p1 = __expf(t1 - m_w), p2 = __expf(t2 - m_w);
            const float l_w = wave_sum((p0 + p1) + p2);
            P[shift + sl] = p0;
            if (v1) P[shift + 32 + sl] = p1;
            if (v2) P[shift + 64 + sl] = p2;
            if (lane == 0) *(LAS f32x2*)(L + L_ML + wave * 8) = f32x2{m_w, l_w};
            LDS_FENCE();
            u32 old = 0;
            if (lane == 0) old = lds_inc(L, 1);
            old = __builtin_amdgcn_readfirstlane(old);
            if (old == 3u) {
              CBAR();
              const LAS f32x2* ml = (const LAS f32x2*)(L + L_ML);
              const f32x2 a0 = ml[0], a1 = ml[1], a2 = ml[2], a3 = ml[3];
              const float M = fmaxf(fmaxf(a0.x, a1.x), fmaxf(a2.x, a3.x));
              const float Lw = (a0.y * __expf(a0.x - M) + a1.y * __expf(a1.x - M)) + (a2.y * __expf(a2.x - M) + a3.y * __expf(a3.x - M));
              if (lane == 0) bst128_sc1(rws, WS_ST + (bh * 8 + spl) * 128, u32x4{__float_as_uint(M), tag, __float_as_uint(Lw), tag});
            }
            STAMP(3);
          }
        } else {
          constexpr int j = i - NS;
          if constexpr ((j & 3) == 0) pv = *(const LAS f32x4*)(P + j);
          const float p = pv[j & 3];
          const f32x2 pp = {p, p};
          acc[0] = __builtin_elementwise_fma(pp, f32x2{blo(kv[i].x), bhi(kv[i].x)}, acc[0]);
          acc[1] = __builtin_elementwise_fma(pp, f32x2{blo(kv[i].y), bhi(kv[i].y)}, acc[1]);
          acc[2] = __builtin_elementwise_fma(pp, f32x2{blo(kv[i].z), bhi(kv[i].z)}, acc[2]);
          acc[3] = __builtin_elementwise_fma(pp, f32x2{blo(kv[i].w), bhi(kv[i].w)}, acc[3]);
          asm volatile("" : "+v"(acc[0]), "+v"(acc[1]), "+v"(acc[2]), "+v"(acc[3]));
        }
      });
      SCHED();
    });
    STAMP(4);
    f32x4 o4;
    o4.x = tr32(acc[0].x, acc[2].x) * scale; o4.y = tr32(acc[0].y, acc[2].y) * scale;
    o4.z = tr32(acc[1].x, acc[3].x) * scale; o4.w = tr32(acc[1].y, acc[3].y) * scale;
    *(LAS f32x4*)(L + L_O + (wave * 256 + c * 8 + half * 4) * 4) = o4;
    wg_barrier();
    STAMP(8);
    if (wave < 2) {
      const LAS f32x2* O2 = (const LAS f32x2*)(L + L_O) + tid;
      const f32x2 s = (O2[0] + O2[128]) + (O2[256] + O2[384]);
      bst128_sc1(rws, WS_O + ((bh * 8 + spl) * 128 + tid) * 16, u32x4{__float_as_uint(s.x), tag, __float_as_uint(s.y), tag});
    }
    STAMP(9);
    if (is_red) {
      const int b = g / 28, cg = g % 28;
      LAS f32x2* red = (LAS f32x2*)(L + L_RED);
      const int l8 = lane & 7;
      float x1a, x1b;
      {
        int vo[4]; bool ac[4] = {true, true, true, true}; u32x4 r[4];
#pragma unroll
        for (int i = 0; i < 4; ++i) vo[i] = WS_Y + ((b * 128 + 32 * wave + 8 * i + (lane >> 3)) * 224 + cg * 8 + l8) * 16;
        poll<4>(rws, vo, ac, tag, r);
        STAMP(10);
        float s0 = (__uint_as_float(r[0].x) + __uint_as_float(r[1].x)) + (__uint_as_float(r[2].x) + __uint_as_float(r[3].x));
        float s1 = (__uint_as_float(r[0].z) + __uint_as_float(r[1].z)) + (__uint_as_float(r[2].z) + __uint_as_float(r[3].z));
        s0 += dpp<ROR8>(s0); s1 += dpp<ROR8>(s1);
        s0 = tr16(s0, s0); s1 = tr16(s1, s1);
        s0 = tr32(s0, s0); s1 = tr32(s1, s1);
        if (lane < 8) red[wave * 8 + lane] = f32x2{s0, s1};
        wg_barrier();
        const f32x2 t = (red[l8] + red[8 + l8]) + (red[16 + l8] + red[24 + l8]);
        const u32 ga = ((const LAS u32*)(L + L_AUX))[l8], xx = ((const LAS u32*)(L + L_AUX + 64))[l8];
        x1a = rbf(blo(xx) + rbf(blo(ga) * rbf(t.x)));
        x1b = rbf(bhi(xx) + rbf(bhi(ga) * rbf(t.y)));
        if (wave == 0) {
          const u32 pk = pack2(x1a, x1b), nb = dppu<XOR1>(pk);
          if (lane < 8 && (lane & 1) == 0) bst128_sc1(rws, WS_X1 + g * 128 + (lane >> 1) * 16, u32x4{pk, tag, nb, tag});
        }
        STAMP(11);
      }
      {
        const bool a1 = lane < 48;
        const int p0 = 14 * wave + (lane >> 3), p1 = 14 * wave + (a1 ? 8 + (lane >> 3) : 0);
        int vo[2] = {WS_F + ((p0 * 2 + b) * 224 + cg * 8 + l8) * 16, WS_F + ((p1 * 2 + b) * 224 + cg * 8 + l8) * 16};
        bool ac[2] = {true, a1}; u32x4 r[2];
        poll<2>(rws, vo, ac, tag, r);
        STAMP(12);
        float s0 = __uint_as_float(r[0].x) + __uint_as_float(r[1].x);
        float s1 = __uint_as_float(r[0].z) + __uint_as_float(r[1].z);
        s0 += dpp<ROR8>(s0); s1 += dpp<ROR8>(s1);
        s0 = tr16(s0, s0); s1 = tr16(s1, s1);
        s0 = tr32(s0, s0); s1 = tr32(s1, s1);
        if (lane < 8) red[32 + wave * 8 + lane] = f32x2{s0, s1};
        wg_barrier();
        if (wave == 0 && lane < 8) {
          const f32x2 t = (red[32 + lane] + red[40 + lane]) + (red[48 + lane] + red[56 + lane]);
          const u32 gb = ((const LAS u32*)(L + L_AUX + 32))[lane];
          ((u32*)pout)[(b * D + cg * 16) / 2 + lane] = pack2(x1a + rbf(blo(gb) * rbf(t.x)), x1b + rbf(bhi(gb) * rbf(t.y)));
          if (g == 0 && lane == 0) __builtin_amdgcn_raw_buffer_store_b32(tag, rws, WS_EPOCH, 0, AUX_SC1);
        }
        STAMP(13);
      }
      return;
    }
    if (!is_ffn) return;
    load_wd(L, wave & 3, lane, wd);
    {
      const int lb = wave >> 1, lh = wave & 1;
      const bool act1 = lane < 48, lact = lh == 0 || act1;
      const int q1 = act1 ? 64 + lane : 0, lrec = lh == 0 ? lane : q1;
      const u32x2 shw = *(const LAS u32x2*)(L + L_LN + lb * 896 + lrec * 8), scw = *(const LAS u32x2*)(L + L_LN + 1792 + lb * 896 + lrec * 8);
      int vo[2] = {WS_X1 + (lb * 28 + (lane >> 2)) * 128 + (lane & 3) * 16, WS_X1 + (lb * 28 + (q1 >> 2)) * 128 + (q1 & 3) * 16};
      bool ac[2] = {true, act1}; u32x4 r[2];
      poll<2>(rws, vo, ac, tag, r);
      STAMP(11);
      const float xf[8] = {blo(r[0].x), bhi(r[0].x), blo(r[0].z), bhi(r[0].z), blo(r[1].x), bhi(r[1].x), blo(r[1].z), bhi(r[1].z)};
      float mean_b, rsd;
      ln_stats(xf, act1, mean_b, rsd);
      const u32 xw0 = lh ? r[1].x : r[0].x, xw1 = lh ? r[1].z : r[0].z;
      const float h0 = rbf(rbf(rbf(blo(xw0) - mean_b) * rsd) * blo(scw.x)) + blo(shw.x), h1 = rbf(rbf(rbf(bhi(xw0) - mean_b) * rsd) * bhi(scw.x)) + bhi(shw.x);
      const float h2 = rbf(rbf(rbf(blo(xw1) - mean_b) * rsd) * blo(scw.y)) + blo(shw.y), h3 = rbf(rbf(rbf(bhi(xw1) - mean_b) * rsd) * bhi(scw.y)) + bhi(shw.y);
      if (lact) *(LAS u32x2*)(L + L_H + lb * 1024 + lrec * 8) = u32x2{pack2(h0, h1), pack2(h2, h3)};
    }
  } else {
    const int a = wave - 4;
    __builtin_amdgcn_s_setprio(3);
    u32 ep = ald32_sc1(ws, WS_EPOCH);
    const bool act1 = lane < 48;
    const int qi = g - G_QP, mi = g - G_MOD;
    const int r0 = 4 * lane, r1 = act1 ? 256 + 4 * lane : 0;
    const int b = lane >> 5, rr = lane & 31;
    const bool actr = rr < 28;
    const int rec = 28 * a + (actr ? rr : 0);
    u32x2 xa0 = {0u, 0u}, xa1 = {0u, 0u}, xb0 = {0u, 0u}, xb1 = {0u, 0u}, xm = {0u, 0u};
    u32 c0 = 0u, c1 = 0u;
    if (is_mod) {
      const int l56 = lane < 56 ? lane : 0;
      c0 = ald32(pc, (112 * a + 2 * l56) * 2); c1 = ald32(pc, (D + 112 * a + 2 * l56) * 2);
    }
    if (is_qp) {
      xa0 = ald64(px, r0 * 2); xa1 = ald64(px, r1 * 2); xb0 = ald64(px, (D + r0) * 2); xb1 = ald64(px, (D + r1) * 2);
      xm = ald64(px, (b * D + 4 * rec) * 2);
    }
    if (is_red) xm = ald64(px, ((g / 28) * D + (g % 28) * 16 + (lane & 3) * 4) * 2);
    if (a == 0 && lane == 0) { flags[0] = 0u; flags[1] = 0u; flags[3] = 0u; flags[4] = 0u; flags[5] = 0u; flags[6] = 0u; flags[7] = 0u; }
    SCHED();
    wg_barrier();
    const unsigned long long t_begin = __builtin_amdgcn_s_memrealtime();
    const int ch = lane & 7, irow = 112 * a + (lane >> 3);
    if (is_qp || is_mu) {
      const char* src = is_qp ? (const char*)pwq + (size_t)irow * (NH * DH * 2) + qi * 128 + ch * 16
                              : (const char*)pwmod + (size_t)irow * (NM * 2) + mi * 128 + ch * 16;
      const int step = is_qp ? 8 * NH * DH * 2 : 8 * NM * 2;
#pragma unroll
      for (int i = 0; i < 14; ++i) dma16(src + (size_t)i * step, lds0 + L_W + (112 * a + 8 * i) * 128);
      asm volatile("s_waitcnt vmcnt(14)" ::: "memory");
    } else {
      asm volatile("s_waitcnt vmcnt(0)" ::: "memory");
    }
    SCHED();
    asm volatile("" : "+v"(ep), "+v"(c0), "+v"(c1), "+v"(xa0), "+v"(xa1), "+v"(xb0), "+v"(xb1), "+v"(xm));
    SCHED();
    tag = __builtin_amdgcn_readfirstlane(ep) + 1u;
    if (is_mod && lane < 56) {
      const float f0 = blo(c0), f1 = bhi(c0), f2 = blo(c1), f3 = bhi(c1);
      *(LAS u32*)(L + L_H + (112 * a + 2 * lane) * 2) = pack2(f0 / (1.f + expf(-f0)), f1 / (1.f + expf(-f1)));
      *(LAS u32*)(L + L_H + 1024 + (112 * a + 2 * lane) * 2) = pack2(f2 / (1.f + expf(-f2)), f3 / (1.f + expf(-f3)));
    }
    if (is_qp || is_mu) {
      float mean_b = 0.f, rsd_b = 0.f;
      if (is_qp) {
        float mean0, sd0, mean1, sd1;
        { const float xf[8] = {blo(xa0.x), bhi(xa0.x), blo(xa0.y), bhi(xa0.y), blo(xa1.x), bhi(xa1.x), blo(xa1.y), bhi(xa1.y)}; ln_stats(xf, act1, mean0, sd0); }
        { const float xf[8] = {blo(xb0.x), bhi(xb0.x), blo(xb0.y), bhi(xb0.y), blo(xb1.x), bhi(xb1.x), blo(xb1.y), bhi(xb1.y)}; ln_stats(xf, act1, mean1, sd1); }
        mean_b = b ? mean1 : mean0; rsd_b = b ? sd1 : sd0;
      }
      CBAR(); WAIT_VM(0); CBAR();
      aux_sync(L, 3, lane);
      STAMP(1);
      load_afrags<128>(L + L_W + 32 * a, lane, af);
      if (is_qp) {
        int po[2] = {WS_MOD + (b * 672 + rec) * 16, WS_MOD + (b * 672 + 112 + rec) * 16};
        bool ac[2] = {actr, actr};
        u32x4 r[2];
        poll<2>(rws, po, ac, tag, r);
        STAMP(2);
        if (actr) {
          u32x2 hv;
          hv.x = pack2(ln_one(blo(xm.x), mean_b, rsd_b, blo(r[0].x), blo(r[1].x)), ln_one(bhi(xm.x), mean_b, rsd_b, bhi(r[0].x), bhi(r[1].x)));
          hv.y = pack2(ln_one(blo(xm.y), mean_b, rsd_b, blo(r[0].z), blo(r[1].z)), ln_one(bhi(xm.y), mean_b, rsd_b, bhi(r[0].z), bhi(r[1].z)));
          *(LAS u32x2*)(L + L_H + b * 1024 + rec * 8) = hv;
        }
        aux_sync(L, 4, lane);
      }
      const f32x4 d = gemv_tile(L + L_H, lane, af);
      if ((lane & 15) < 2) {
        const int ro = (is_mu ? WS_MOD + (lane & 1) * (672 * 16) + mi * 256 : WS_Q + (lane & 1) * (1024 * 16) + qi * 256) + (a * 4 + (lane >> 4)) * 16;
        bst128_sc1(rws, ro, u32x4{pack2(d.x, d.y), tag, pack2(d.z, d.w), tag});
      }
      STAMP(3);
      aux_sync(L, 6, lane);
      if (a == 0 && lane == 0) flags[5] = 1u;
    }
    auto late_img = [&]() {
      const char* src = is_mod ? (const char*)pwmod + (size_t)irow * (NM * 2) + (14 + mi) * 128 + ch * 16
                               : (const char*)((ch & 1) ? pwu : pwg) + (size_t)irow * (FF * 2) + fcg * 64 + (ch >> 1) * 16;
      const int step = is_mod ? 8 * NM * 2 : 8 * FF * 2;
#pragma unroll
      for (int i = 0; i < 14; ++i) dma16(src + (size_t)i * step, lds0 + L_W + (112 * a + 8 * i) * 128);
      if (is_ffn) {
#pragma unroll
        for (int i = 0; i < 7; ++i) dma16((const char*)pwd + (size_t)fcg * 32 * (D * 2) + (a * 7 + i) * 1024 + lane * 16, lds0 + L_W2 + (a * 7 + i) * 1024);
      }
    };
    if (!is_qp && (a == 0 || is_mod || is_ffn)) {
      const unsigned lim = a == 0 ? QSLEEP_ : IMGDELAY_;
      for (int i = 0; i < 4096 && __builtin_amdgcn_s_memrealtime() - t_begin < lim; ++i) __builtin_amdgcn_s_sleep(4);
    }
    if ((is_mod || is_ffn) && a != 0) late_img();
    if (a == 0) {
      int vo[1] = {WS_Q + (bat * 1024 + head * 64 + lane) * 16};
      bool ac[1] = {true};
      u32x4 r[1];
      poll<1>(rws, vo, ac, tag, r);
      u32x2 qq = {r[0].x, r[0].z};
      *(LAS u32x2*)(L + L_Q + lane * 8) = qq;
      if (lane == 0) flags[2] = tag;
      LDS_FENCE();
      if (lane == 0) flags[0] = 1u;
      STAMP(6);
      if (is_mod || is_ffn) late_img();
    }
    if (is_mod || is_ffn) {
      CBAR(); WAIT_VM(0); CBAR();
      aux_sync(L, 7, lane);
      STAMP(4);
      load_afrags<128>(L + L_W + 32 * a, lane, af);
      if (is_mod) {
        const f32x4 d = gemv_tile(L + L_H, lane, af);
        if ((lane & 15) < 2)
          bst128_sc1(rws, WS_MOD + (lane & 1) * (672 * 16) + (14 + mi) * 256 + (a * 4 + (lane >> 4)) * 16, u32x4{pack2(d.x, d.y), tag, pack2(d.z, d.w), tag});
        STAMP(5);
      }
    }
    if (a != 0) lds_wait(flags + 0);
    u32 wo[32];
    {
      const rsrc_t rwo = mk_rsrc(pwo, NH * DH * D * 2);
      const int l56 = lane < 56 ? lane : 0;
      const int wbase = (head * DH + spl * 32) * (D * 2);
#pragma unroll
      for (int i = 0; i < 32; ++i) {
        if (i >= 8) WAIT_VM(7);
        wo[i] = bld32<AUX_NT>(rwo, (112 * a + 2 * l56) * 2, wbase + i * (D * 2));
        SCHED();
      }
#pragma unroll
      for (int p = 0; p < 16; ++p) {
        const u32 lo = perm_lo(wo[2 * p], wo[2 * p + 1]), hi = perm_hi(wo[2 * p], wo[2 * p + 1]);
        wo[2 * p] = lo; wo[2 * p + 1] = hi;
        PIN2(wo[2 * p], wo[2 * p + 1]);
      }
    }
    if (is_red && a == 1) {
      const int rb = g / 28, rcg = g % 28;
      int vo[1] = {WS_MOD + (rb * 672 + ((lane & 4) ? 560 : 224) + rcg * 4 + (lane & 3)) * 16};
      bool ac[1] = {lane < 8};
      u32x4 r[1];
      poll<1, 8>(rws, vo, ac, tag, r);
      if (lane < 8) *(LAS u32x2*)(L + L_AUX + lane * 8) = u32x2{r[0].x, r[0].z};
      if (lane < 4) *(LAS u32x2*)(L + L_AUX + 64 + lane * 8) = xm;
    }
    if (is_ffn) {
      const int lb = a >> 1, lh = a & 1;
      const bool lact = lh == 0 || act1;
      const int lrec = lh == 0 ? lane : (act1 ? 64 + lane : 0);
      int vo[2] = {WS_MOD + (lb * 672 + 336 + lrec) * 16, WS_MOD + (lb * 672 + 448 + lrec) * 16};
      bool ac[2] = {lact, lact};
      u32x4 md[2];
      poll<2, 8>(rws, vo, ac, tag, md);
      if (lact) {
        *(LAS u32x2*)(L + L_LN + lb * 896 + lrec * 8) = u32x2{md[0].x, md[0].z};
        *(LAS u32x2*)(L + L_LN + 1792 + lb * 896 + lrec * 8) = u32x2{pack2(1.f + blo(md[1].x), 1.f + bhi(md[1].x)), pack2(1.f + blo(md[1].z), 1.f + bhi(md[1].z))};
      }
      load_wd(L, a, lane, wd);
    }
    STAMP(7);
    wg_barrier();
    STAMP(8);
    {
      float wgt;
      {
        int vo[1] = {WS_ST + (bh * 8 + (lane >> 3)) * 128};
        bool ac[1] = {true};
        u32x4 r[1];
        poll<1>(rws, vo, ac, tag, r);
        const float ms = __uint_as_float(r[0].x), ls = __uint_as_float(r[0].z);
        const float M = max32(max16(fmaxf(ms, dpp<ROR8>(ms))));
        const float e = __expf(ms - M);
        float t = e * ls;
        t += dpp<ROR8>(t); t = tr16(t, t); t = tr32(t, t);
        wgt = e / t;
        asm volatile("" : "+v"(wgt));
      }
      const int ob = WS_O + ((bh * 8 + (lane >> 3)) * 128 + spl * 16 + (lane & 7)) * 16;
      int vo[2] = {ob, ob + 128};
      bool ac[2] = {true, true};
      u32x4 r[2];
      poll<2>(rws, vo, ac, tag, r);
      STAMP(9);
      float s0 = __uint_as_float(r[0].x) * wgt, s1 = __uint_as_float(r[0].z) * wgt, s2 = __uint_as_float(r[1].x) * wgt, s3 = __uint_as_float(r[1].z) * wgt;
      s0 += dpp<ROR8>(s0); s1 += dpp<ROR8>(s1); s2 += dpp<ROR8>(s2); s3 += dpp<ROR8>(s3);
      s0 = tr16(s0, s2); s1 = tr16(s1, s3);
      s0 = tr32(s0, s0); s1 = tr32(s1, s1);
      const u32 op = pack2(s0, s1);
      float y0 = 0.f, y1 = 0.f, y2 = 0.f, y3 = 0.f;
#pragma unroll
      for (int p = 0; p < 8; ++p) {
        const u32 oa = (u32)__builtin_amdgcn_readlane((int)op, p), oc = (u32)__builtin_amdgcn_readlane((int)op, 16 + p);
        y0 = dot2(wo[2 * p], oa, y0); y1 = dot2(wo[2 * p + 1], oa, y1);
        y2 = dot2(wo[16 + 2 * p], oc, y2); y3 = dot2(wo[16 + 2 * p + 1], oc, y3);
      }
      if (lane < 56) bst128_sc1(rws, WS_Y + ((bat * 128 + head * 8 + spl) * 224 + 56 * a + lane) * 16, u32x4{__float_as_uint(y0 + y2), tag, __float_as_uint(y1 + y3), tag});
      STAMP(10);
    }
    if (!is_ffn) return;
  }

  {
    const int db = wave >> 2, dq = wave & 3;
    wg_barrier();
    STAMP(12);
    if (wave >= 4) {
      const f32x4 d = gemv_tile(L + L_H, lane, af);
      float ak[4];
#pragma unroll
      for (int i = 0; i < 4; ++i) {
        auto s = __builtin_amdgcn_permlane32_swap(__float_as_uint(d[i]), __float_as_uint(d[i]), false, false);
        const float gg = rbf(__uint_as_float(s[0])), uu = rbf(__uint_as_float(s[1]));
        const float z = 0.7978845608028654f * (gg + 0.044715f * gg * gg * gg);
        const float th = 1.f - 2.f * __builtin_amdgcn_rcpf(__expf(2.f * z) + 1.f);
        ak[i] = rbf(0.5f * gg * (1.f + th)) * uu;
      }
      if (lane < 32 && (lane & 15) < 2)
        *(LAS u32x2*)(L + L_ACT + (lane & 1) * 64 + (8 * (wave - 4) + 4 * (lane >> 4)) * 2) = u32x2{pack2(ak[0], ak[1]), pack2(ak[2], ak[3])};
    }
    wg_barrier();
    STAMP(13);
    const LAS u32x4* ap = (const LAS u32x4*)(L + L_ACT + db * 64);
    const u32x4 a0 = ap[0], a1 = ap[1], a2 = ap[2], a3 = ap[3];
    const u32 av[16] = {a0.x, a0.y, a0.z, a0.w, a1.x, a1.y, a1.z, a1.w, a2.x, a2.y, a2.z, a2.w, a3.x, a3.y, a3.z, a3.w};
    float y0 = 0.f, y1 = 0.f, y2 = 0.f, y3 = 0.f;
#pragma unroll
    for (int p = 0; p < 8; ++p) {
      y0 = dot2(wd[2 * p], av[p], y0); y1 = dot2(wd[2 * p + 1], av[p], y1);
      y2 = dot2(wd[16 + 2 * p], av[8 + p], y2); y3 = dot2(wd[16 + 2 * p + 1], av[8 + p], y3);
    }
    if (lane < 56) bst128_sc1(rws, WS_F + ((fi * 2 + db) * 224 + 56 * dq + lane) * 16, u32x4{__float_as_uint(y0 + y2), tag, __float_as_uint(y1 + y3), tag});
    STAMP(14);
  }
}

extern "C" void kernel_launch(void* const* d_in, const int* in_sizes, int n_in,
                              void* d_out, int out_size, void* d_ws, size_t ws_size,
                              hipStream_t stream) {
  fused_decode_kernel<<<NWG, NTHR, 0, stream>>>(d_in[1], d_in[2], d_ws, d_in[0], d_in[9], d_in[10],
                                                d_in[3], d_in[4], d_in[6], d_in[7], d_in[8], d_out);
}
```

```cpp
#include <hip/hip_runtime.h>
#include <stdint.h>
#pragma clang fp contract(off)

typedef unsigned u32;
typedef unsigned short u16;
typedef u32 u32x4 __attribute__((ext_vector_type(4)));
typedef u32 u32x2 __attribute__((ext_vector_type(2)));
typedef float f32x4 __attribute__((ext_vector_type(4)));
typedef float f32x2 __attribute__((ext_vector_type(2)));
typedef __bf16 bf16x2 __attribute__((ext_vector_type(2)));
typedef short s16x4 __attribute__((ext_vector_type(4)));
typedef short s16x8 __attribute__((ext_vector_type(8)));
typedef __bf16 bf16x8 __attribute__((ext_vector_type(8)));
typedef __amdgpu_buffer_rsrc_t rsrc_t;
#define LAS __attribute__((address_space(3)))

namespace {

constexpr int D = 448, NH = 16, SK = 4096, DH = 256, FF = 1792, NM = 6 * D;
constexpr int NWG = 256, NTHR = 512;
constexpr int NS = 80;
constexpr int RING = 56;
constexpr int NOV = 12, NPRE = RING + NOV;
constexpr int WIN = 8;
constexpr int NSA = 76, NSF = 60, NSM = 60, NSQ = 52;
static_assert(3 * NSA + 2 * NSF + NSM + 2 * NSQ == 512 && NSA <= NS && NPRE <= NSA, "");
constexpr int PREQ = 24;
constexpr unsigned T_QPOLL = 400, T_IMG = 600, T_WO = 400;
constexpr int G_RED = 0, N_RED = 56;
constexpr int G_FFN = 96, N_FFN = 56;
constexpr int G_MOD = 160, N_MOD = 28;
constexpr int G_QP = 192, N_QP = 64;

constexpr int WS_EPOCH = 0;
constexpr int WS_MOD = 4096;
constexpr int WS_MU = 32768;
constexpr int WS_Q = 65536;
constexpr int WS_ST = WS_Q + 2 * 1024 * 16;
constexpr int WS_X1 = WS_ST + 256 * 128;
constexpr int WS_O = WS_X1 + 8192;
constexpr int WS_Y = WS_O + 256 * 128 * 16;
constexpr int WS_F = WS_Y + 256 * 224 * 16;
constexpr int WS_END = WS_F + N_FFN * 2 * 224 * 16;

constexpr int AUX_NT = 2, AUX_SC1 = 16;
constexpr u32 SPIN_CAP = 1u << 22;

constexpr int L_FLAG = 0;
constexpr int L_ML = 64;
constexpr int L_AUX = 128;
constexpr int L_Q = 256;
constexpr int L_P = L_Q + 512;
constexpr int L_O = L_P + 8 * NS * 4;
constexpr int L_H = L_O + 4096;
constexpr int L_RED = L_H + 2048;
constexpr int L_ACT = L_RED + 1024;
constexpr int L_LN = L_ACT + 256;
constexpr int L_W = L_LN + 3584;
constexpr int L_W2 = L_W + 448 * 128;
constexpr int L_OV = L_W2 + 32 * 896;
constexpr int L_END = L_OV + 4 * NOV * 1024;
static_assert(L_END <= 160 * 1024 && (L_O % 16) == 0 && (L_W % 16) == 0, "");

__device__ __forceinline__ rsrc_t mk_rsrc(const void* p, u32 bytes) {
  return __builtin_amdgcn_make_buffer_rsrc((void*)p, 0, (int)bytes, 0x00020000);
}
template <int AUX> __device__ __forceinline__ u32x4 bld128(rsrc_t r, int voff, int soff) {
  return __builtin_amdgcn_raw_buffer_load_b128(r, voff, soff, AUX);
}
template <int AUX> __device__ __forceinline__ u32 bld32(rsrc_t r, int voff, int soff) {
  return __builtin_amdgcn_raw_buffer_load_b32(r, voff, soff, AUX);
}
__device__ __forceinline__ void bst128_sc1(rsrc_t r, int voff, u32x4 v) {
  __builtin_amdgcn_raw_buffer_store_b128(v, r, voff, 0, AUX_SC1);
}
__device__ __forceinline__ float dot2(u32 a, u32 b, float c) {
  return __builtin_amdgcn_fdot2_f32_bf16(__builtin_bit_cast(bf16x2, a), __builtin_bit_cast(bf16x2, b), c, false);
}
__device__ __forceinline__ u32 pack2(float lo, float hi) {
  bf16x2 v = {(__bf16)lo, (__bf16)hi};
  return __builtin_bit_cast(u32, v);
}
__device__ __forceinline__ float rbf(float f) { return (float)(__bf16)f; }
__device__ __forceinline__ float blo(u32 v) { return __uint_as_float(v << 16); }
__device__ __forceinline__ float bhi(u32 v) { return __uint_as_float(v & 0xffff0000u); }
__device__ __forceinline__ u32 perm_lo(u32 a, u32 b) { return __builtin_amdgcn_perm(b, a, 0x05040100u); }
__device__ __forceinline__ u32 perm_hi(u32 a, u32 b) { return __builtin_amdgcn_perm(b, a, 0x07060302u); }
template <int CTRL> __device__ __forceinline__ float dpp(float x) {
  return __builtin_bit_cast(float, __builtin_amdgcn_mov_dpp(__builtin_bit_cast(int, x), CTRL, 0xf, 0xf, true));
}
template <int CTRL> __device__ __forceinline__ u32 dppu(u32 x) {
  return (u32)__builtin_amdgcn_mov_dpp((int)x, CTRL, 0xf, 0xf, true);
}
constexpr int XOR1 = 0xB1, XOR2 = 0x4E, XOR7 = 0x141, ROR8 = 0x128, ROR4 = 0x124, ROR2 = 0x122, ROR1 = 0x121;
__device__ __forceinline__ float tr32(float a, float b) {
  auto s = __builtin_amdgcn_permlane32_swap(__float_as_uint(a), __float_as_uint(b), false, false);
  return __uint_as_float(s[0]) + __uint_as_float(s[1]);
}
__device__ __forceinline__ float tr16(float a, float b) {
  auto s = __builtin_amdgcn_permlane16_swap(__float_as_uint(a), __float_as_uint(b), false, false);
  return __uint_as_float(s[0]) + __uint_as_float(s[1]);
}
__device__ __forceinline__ float max16(float x) {
  auto s = __builtin_amdgcn_permlane16_swap(__float_as_uint(x), __float_as_uint(x), false, false);
  return fmaxf(__uint_as_float(s[0]), __uint_as_float(s[1]));
}
__device__ __forceinline__ float max32(float x) {
  auto s = __builtin_amdgcn_permlane32_swap(__float_as_uint(x), __float_as_uint(x), false, false);
  return fmaxf(__uint_as_float(s[0]), __uint_as_float(s[1]));
}
__device__ __forceinline__ float row_sum(float x) {
  x += dpp<ROR8>(x); x += dpp<ROR4>(x); x += dpp<ROR2>(x); x += dpp<ROR1>(x);
  return x;
}
__device__ __forceinline__ float wave_sum(float x) { x = row_sum(x); x = tr16(x, x); return tr32(x, x); }
__device__ __forceinline__ float wave_max(float x) {
  x = fmaxf(x, dpp<ROR8>(x)); x = fmaxf(x, dpp<ROR4>(x)); x = fmaxf(x, dpp<ROR2>(x)); x = fmaxf(x, dpp<ROR1>(x));
  return max32(max16(x));
}
constexpr int vmcnt_imm(int n) { return (n & 15) | ((n >> 4) << 14) | (7 << 4) | (15 << 8); }
#define WAIT_VM(n) __builtin_amdgcn_s_waitcnt(vmcnt_imm(n))
#define WAIT_LGKM0() __builtin_amdgcn_s_waitcnt(0xC07F)
#define SCHED() __builtin_amdgcn_sched_barrier(0)
#define CBAR() asm volatile("" ::: "memory")
#define LDS_FENCE() do { CBAR(); WAIT_LGKM0(); CBAR(); } while (0)
#define DRAIN() asm volatile("s_waitcnt vmcnt(0)" ::: "memory")
#define PIN2(a, b) asm volatile("" : "+v"(a), "+v"(b))
__device__ __forceinline__ void wg_barrier() { LDS_FENCE(); __builtin_amdgcn_s_barrier(); CBAR(); }


template <int N, int SLEEP = 0>
__device__ __forceinline__ void poll(rsrc_t ws, const int (&voff)[N], const bool (&act)[N], u32 tag, u32x4 (&r)[N]) {
  bool ok[N];
#pragma unroll
  for (int i = 0; i < N; ++i) { ok[i] = !act[i]; r[i] = u32x4{0u, 0u, 0u, 0u}; }
  for (u32 spin = 0; spin < SPIN_CAP; ++spin) {
    CBAR();
#pragma unroll
    for (int i = 0; i < N; ++i) if (!ok[i]) r[i] = bld128<AUX_SC1>(ws, voff[i], 0);
    bool all = true;
#pragma unroll
    for (int i = 0; i < N; ++i) { ok[i] = ok[i] || (r[i].y == tag && r[i].w == tag); all = all && ok[i]; }
    if (__all(all)) break;
    if (SLEEP) __builtin_amdgcn_s_sleep(SLEEP);
  }
  CBAR();
}
__device__ __forceinline__ void lds_wait(volatile LAS u32* f, u32 want = 1u) {
  for (u32 spin = 0; spin < SPIN_CAP; ++spin) {
    if (*f >= want) break;
    __builtin_amdgcn_s_sleep(1);
  }
  CBAR();
}
__device__ __forceinline__ u32 lds_inc(LAS unsigned char* L, int idx) {
  return __hip_atomic_fetch_add((LAS u32*)(L + L_FLAG) + idx, 1u, __ATOMIC_RELAXED, __HIP_MEMORY_SCOPE_WORKGROUP);
}
__device__ __forceinline__ void aux_sync(LAS unsigned char* L, int idx, int lane) {
  LDS_FENCE();
  if (lane == 0) lds_inc(L, idx);
  volatile LAS u32* f = (volatile LAS u32*)(L + L_FLAG) + idx;
  for (u32 spin = 0; spin < SPIN_CAP; ++spin) if (*f >= 4u) break;
  CBAR();
}

__device__ __forceinline__ void ln_stats(const float (&x)[8], bool act1, float& mean_b, float& rsd) {
  float s = (x[0] + x[1]) + (x[2] + x[3]);
  float s1 = (x[4] + x[5]) + (x[6] + x[7]);
  s += act1 ? s1 : 0.f;
  const float mean = wave_sum(s) * (1.0f / D);
  float v = 0.f, v1 = 0.f;
#pragma unroll
  for (int i = 0; i < 4; ++i) { float d = x[i] - mean; v = fmaf(d, d, v); float e = x[4 + i] - mean; v1 = fmaf(e, e, v1); }
  v += act1 ? v1 : 0.f;
  const float var = wave_sum(v) * (1.0f / D);
  mean_b = rbf(mean);
  rsd = __builtin_amdgcn_rcpf(rbf(__builtin_amdgcn_sqrtf(rbf(rbf(var) + 1e-5f))));
}
__device__ __forceinline__ void ln_stats1(const float (&x)[8], bool act1, float& mean_b, float& rsd) {
  const float pv = __builtin_bit_cast(float, __builtin_amdgcn_readfirstlane(__builtin_bit_cast(int, x[0])));
  float d[8];
#pragma unroll
  for (int i = 0; i < 8; ++i) d[i] = x[i] - pv;
  float s = (d[0] + d[1]) + (d[2] + d[3]), s1 = (d[4] + d[5]) + (d[6] + d[7]);
  float v = fmaf(d[0], d[0], d[1] * d[1]) + fmaf(d[2], d[2], d[3] * d[3]), v1 = fmaf(d[4], d[4], d[5] * d[5]) + fmaf(d[6], d[6], d[7] * d[7]);
  s += act1 ? s1 : 0.f; v += act1 ? v1 : 0.f;
  s += dpp<ROR8>(s); v += dpp<ROR8>(v); s += dpp<ROR4>(s); v += dpp<ROR4>(v);
  s += dpp<ROR2>(s); v += dpp<ROR2>(v); s += dpp<ROR1>(s); v += dpp<ROR1>(v);
  s = tr16(s, s); v = tr16(v, v); s = tr32(s, s); v = tr32(v, v);
  const float md = s * (1.0f / D);
  const float var = fmaxf(fmaf(-md, md, v * (1.0f / D)), 0.f);
  mean_b = rbf(pv + md);
  rsd = __builtin_amdgcn_rcpf(rbf(__builtin_amdgcn_sqrtf(rbf(rbf(var) + 1e-5f))));
}
template <int I> struct ic { static constexpr int value = I; };
template <int B, int E, class F> __device__ __forceinline__ void static_for(F&& f) {
  if constexpr (B < E) { f(ic<B>{}); static_for<B + 1, E>(f); }
}
__device__ __forceinline__ void dma16_buf(u32x4 desc, int voff, u32 lds_dst) {
  unsigned keep;
  asm volatile("s_mov_b32 %0, m0\n\ts_mov_b32 m0, %3\n\ts_nop 0\n\tbuffer_load_dwordx4 %1, %2, 0 offen nt lds\n\ts_mov_b32 m0, %0"
               : "=&s"(keep) : "v"(voff), "s"(desc), "s"(lds_dst) : "memory");
}
__device__ __forceinline__ void dma16(const void* src, u32 lds_dst) {
  unsigned keep;
  asm volatile("s_mov_b32 %0, m0\n\ts_mov_b32 m0, %2\n\ts_nop 0\n\tglobal_load_lds_dwordx4 %1, off nt\n\ts_mov_b32 m0, %0"
               : "=&s"(keep) : "v"(src), "s"(lds_dst) : "memory");
}
__device__ __forceinline__ u32 ald32(const void* base, int voff) {
  u32 r; asm volatile("global_load_dword %0, %1, %2 nt" : "=v"(r) : "v"(voff), "s"(base) : "memory"); return r;
}
__device__ __forceinline__ u32 ald32_sc1(const void* base, int voff) {
  u32 r; asm volatile("global_load_dword %0, %1, %2 sc1" : "=v"(r) : "v"(voff), "s"(base) : "memory"); return r;
}
__device__ __forceinline__ u32x2 ald64(const void* base, int voff) {
  u32x2 r; asm volatile("global_load_dwordx2 %0, %1, %2 nt" : "=v"(r) : "v"(voff), "s"(base) : "memory"); return r;
}
__device__ __forceinline__ int img_swz(int row) { return ((row >> 1) & 1) | ((row >> 3) & 1) << 1; }
template <int NK>
__device__ __forceinline__ void load_afrags(LAS unsigned char* img, int tile, int lane, s16x8* af) {
  const int r = 8 * (lane >> 4) + ((lane & 15) >> 2);
  LAS unsigned char* p0 = img + r * 128 + ((tile ^ img_swz(r)) * 32) + (lane & 3) * 8;
  LAS unsigned char* p1 = img + (r + 4) * 128 + ((tile ^ img_swz(r + 4)) * 32) + (lane & 3) * 8;
#pragma unroll
  for (int s = 0; s < NK; ++s) {
    const s16x4 lo = __builtin_amdgcn_ds_read_tr16_b64_v4i16((LAS s16x4*)(p0 + s * 4096));
    const s16x4 hi = __builtin_amdgcn_ds_read_tr16_b64_v4i16((LAS s16x4*)(p1 + s * 4096));
    af[s] = __builtin_shufflevector(lo, hi, 0, 1, 2, 3, 4, 5, 6, 7);
    asm volatile("" : "+v"(af[s]));
  }
}
template <int NK>
__device__ __forceinline__ f32x4 gemv_tile(LAS unsigned char* h, int lane, const s16x8* af) {
  const LAS u32x4* hp = (const LAS u32x4*)(h + (lane & 1) * 1024 + (lane >> 4) * 16);
  u32x4 bf[14];
#pragma unroll
  for (int s = 0; s < NK; ++s) bf[s] = hp[s * 4];
  f32x4 acc0 = {0.f, 0.f, 0.f, 0.f}, acc1 = {0.f, 0.f, 0.f, 0.f};
#pragma unroll
  for (int s = 0; s < NK; s += 2) {
    acc0 = __builtin_amdgcn_mfma_f32_16x16x32_bf16(__builtin_bit_cast(bf16x8, af[s]), __builtin_bit_cast(bf16x8, bf[s]), acc0, 0, 0, 0);
    if (s + 1 < NK) acc1 = __builtin_amdgcn_mfma_f32_16x16x32_bf16(__builtin_bit_cast(bf16x8, af[s + 1]), __builtin_bit_cast(bf16x8, bf[s + 1]), acc1, 0, 0, 0);
  }
  return acc0 + acc1;
}
__device__ __forceinline__ void load_wd(LAS unsigned char* L, int dq, int lane, u32 (&wd)[32]) {
  const int l56 = lane < 56 ? lane : 0;
  const LAS u32* wp = (const LAS u32*)(L + L_W2 + (112 * dq + 2 * l56) * 2);
#pragma unroll
  for (int p = 0; p < 16; ++p) {
    const u32 ra = wp[(2 * p) * 224], rb = wp[(2 * p + 1) * 224];
    wd[2 * p] = perm_lo(ra, rb); wd[2 * p + 1] = perm_hi(ra, rb);
    PIN2(wd[2 * p], wd[2 * p + 1]);
  }
}

template <int N, int PRE, int W> constexpr int cons_upto(int t) {
  if (t < 0) return 0;
  if (PRE + t >= 2 * N) return 2 * N;
  int a = 4 * (t + 1), b = PRE + t + 1 - W;
  return a < b ? a : (b > 0 ? b : 0);
}
template <int N, int PRE, int W>
__device__ __forceinline__ void stream_kv(LAS unsigned char* L, rsrc_t rws, const char* kb, const char* vb, int wave, int lane, int st_off,
                                          u32& tag, f32x2 (&acc)[4], float& scale) {
  static_assert(PRE <= N && PRE <= NPRE && N <= NS && N > 32, "");
  constexpr int PADN = (N + 15) / 16 * 16;
  volatile LAS u32* flags = (volatile LAS u32*)(L + L_FLAG);
  const rsrc_t rK = mk_rsrc(kb, N * 4096), rV = mk_rsrc(vb, N * 4096);
  const int voff = wave * 1024 + lane * 16;
  const int half = lane >> 5, c = lane & 31;
  LAS float* P = (LAS float*)(L + L_P) + (wave * 2 + half) * NS;
  u32x4 kv[2 * N];
#pragma unroll
  for (int i = 0; i < (PRE < RING ? PRE : RING); ++i) {
    if (i >= W) WAIT_VM(W - 1);
    kv[i] = bld128<AUX_NT>(rK, voff, i * 4096);
    SCHED();
  }
  if constexpr (PRE > RING) {
    const u32x4 desc = {(u32)(uintptr_t)kb, (u32)((uintptr_t)kb >> 32), (u32)(N * 4096), 0x00020000u};
#pragma unroll
    for (int e = RING; e < PRE; ++e) {
      WAIT_VM(W - 1);
      dma16_buf(desc, voff + e * 4096, (u32)(uintptr_t)L + L_OV + (wave * NOV + e - RING) * 1024);
      SCHED();
    }
  }
  const LAS u32x4* ovp = (const LAS u32x4*)(L + L_OV + wave * (NOV * 1024)) + lane;
  lds_wait(flags + 0);
  tag = flags[2];
  const u32x4 q = *(const LAS u32x4*)(L + L_Q + c * 16);
  float lvl[5];
  float sc[3] = {0.f, 0.f, 0.f};
#pragma unroll
  for (int k = 0; k < 4; ++k) acc[k] = f32x2{0.f, 0.f};
  float m_w = 0.f;
  f32x4 pv = {0.f, 0.f, 0.f, 0.f};
  auto tree = [&](auto icn, float d) {
    constexpr int i = decltype(icn)::value;
    constexpr int j = i & 31;
    if constexpr ((j & 1) == 0) lvl[0] = d;
    else {
      float x = tr16(lvl[0], d);
      if constexpr ((j & 2) == 0) lvl[1] = x;
      else {
        float a = lvl[1] + dpp<ROR8>(lvl[1]), b = x + dpp<ROR8>(x);
        x = (lane & 8) ? b : a;
        if constexpr ((j & 4) == 0) lvl[2] = x;
        else {
          a = lvl[2] + dpp<XOR7>(lvl[2]); b = x + dpp<XOR7>(x);
          x = (lane & 4) ? b : a;
          if constexpr ((j & 8) == 0) lvl[3] = x;
          else {
            a = lvl[3] + dpp<XOR2>(lvl[3]); b = x + dpp<XOR2>(x);
            x = (lane & 2) ? b : a;
            if constexpr (j == 15 && i == PADN - 1) sc[i >> 5] = x + dpp<XOR1>(x);
            else if constexpr ((j & 16) == 0) lvl[4] = x;
            else {
              a = lvl[4] + dpp<XOR1>(lvl[4]); b = x + dpp<XOR1>(x);
              sc[i >> 5] = (lane & 1) ? b : a;
            }
          }
        }
      }
    }
  };
  static_for<0, 2 * N - PRE + 1>([&](auto tc) {
    constexpr int t = decltype(tc)::value;
    constexpr int e = PRE + t;
    if constexpr (e < 2 * N) {
      if constexpr (e >= W) WAIT_VM(W - 1);
      if constexpr (e < N) kv[e] = bld128<AUX_NT>(rK, voff, e * 4096);
      else kv[e] = bld128<AUX_NT>(rV, voff, (e - N) * 4096);
      SCHED();
    }
    if constexpr (cons_upto<N, PRE, W>(t - 1) <= N + 20 && cons_upto<N, PRE, W>(t) > N + 20) {
      lds_wait(flags + 1, 4u);
      const LAS f32x2* ml = (const LAS f32x2*)(L + L_ML);
      const float M = fmaxf(fmaxf(ml[0].x, ml[1].x), fmaxf(ml[2].x, ml[3].x));
      scale = __expf(m_w - M);
      asm volatile("" : "+v"(scale));
    }
    static_for<cons_upto<N, PRE, W>(t - 1), cons_upto<N, PRE, W>(t)>([&](auto icn) {
      constexpr int i = decltype(icn)::value;
      if constexpr (i == RING && PRE > RING) asm volatile("s_waitcnt vmcnt(%0)" :: "n"(W) : "memory");
      if constexpr (i >= RING && i < PRE) kv[i] = ovp[(i - RING) * 64];
      if constexpr (i < N) {
        float d = dot2(kv[i].x, q.x, 0.f);
        d = dot2(kv[i].y, q.y, d); d = dot2(kv[i].z, q.z, d); d = dot2(kv[i].w, q.w, d);
        tree(icn, d);
        if constexpr (i == N - 1) {
          static_for<N, PADN>([&](auto pc_) { tree(pc_, 0.f); });
          const int sl = ((lane >> 4) & 1) | ((lane >> 3) & 1) << 1 | ((lane >> 2) & 1) << 2 | ((lane >> 1) & 1) << 3 | (lane & 1) << 4;
          const bool v1 = 32 + sl < N, v2 = 64 + sl < N;
          const float t0 = rbf(sc[0]) * 0.0625f;
          const float t1 = v1 ? rbf(sc[1]) * 0.0625f : -INFINITY, t2 = v2 ? rbf(sc[2]) * 0.0625f : -INFINITY;
          m_w = wave_max(fmaxf(fmaxf(t0, t1), t2));
          const float p0 = __expf(t0 - m_w), p1 = __expf(t1 - m_w), p2 = __expf(t2 - m_w);
          const float l_w = wave_sum((p0 + p1) + p2);
          P[sl] = p0;
          if (v1) P[32 + sl] = p1;
          if (v2) P[64 + sl] = p2;
          if (lane == 0) *(LAS f32x2*)(L + L_ML + wave * 8) = f32x2{m_w, l_w};
          LDS_FENCE();
          u32 old = 0;
          if (lane == 0) old = lds_inc(L, 1);
          old = __builtin_amdgcn_readfirstlane(old);
          if (old == 3u) {
            CBAR();
            const LAS f32x2* ml = (const LAS f32x2*)(L + L_ML);
            const f32x2 a0 = ml[0], a1 = ml[1], a2 = ml[2], a3 = ml[3];
            const float M = fmaxf(fmaxf(a0.x, a1.x), fmaxf(a2.x, a3.x));
            const float Lw = (a0.y * __expf(a0.x - M) + a1.y * __expf(a1.x - M)) + (a2.y * __expf(a2.x - M) + a3.y * __expf(a3.x - M));
            if (lane == 0) bst128_sc1(rws, st_off, u32x4{__float_as_uint(M), tag, __float_as_uint(Lw), tag});
          }
        }
      } else {
        constexpr int j = i - N;
        if constexpr ((j & 3) == 0) pv = *(const LAS f32x4*)(P + j);
        const float p = pv[j & 3];
        const f32x2 pp = {p, p};
        acc[0] = __builtin_elementwise_fma(pp, f32x2{blo(kv[i].x), bhi(kv[i].x)}, acc[0]);
        acc[1] = __builtin_elementwise_fma(pp, f32x2{blo(kv[i].y), bhi(kv[i].y)}, acc[1]);
        acc[2] = __builtin_elementwise_fma(pp, f32x2{blo(kv[i].z), bhi(kv[i].z)}, acc[2]);
        acc[3] = __builtin_elementwise_fma(pp, f32x2{blo(kv[i].w), bhi(kv[i].w)}, acc[3]);
        asm volatile("" : "+v"(acc[0]), "+v"(acc[1]), "+v"(acc[2]), "+v"(acc[3]));
      }
    });
    SCHED();
  });
}

}

__global__ void __launch_bounds__(NTHR, 2)
fused_decode_kernel(const void* pK, const void* pV, void* ws, const void* px, const void* pc, const void* pwmod,
                    const void* pwq, const void* pwo, const void* pwg, const void* pwu, const void* pwd, void* pout) {
  asm volatile("" : "+s"(pK), "+s"(pV), "+s"(ws), "+s"(px), "+s"(pc), "+s"(pwmod), "+s"(pwq), "+s"(pwo), "+s"(pwg), "+s"(pwu), "+s"(pwd), "+s"(pout));
  __shared__ __attribute__((aligned(16))) unsigned char lds_raw[L_END];
  LAS unsigned char* L = (LAS unsigned char*)lds_raw;
  const int tid = threadIdx.x, lane = tid & 63;
  const int wave = __builtin_amdgcn_readfirstlane(tid >> 6);
  const int g = blockIdx.x;
  const int head = 2 * (g & 7) + ((g >> 3) & 1), bat = (g >> 4) & 1, spl = g >> 5;
  const int bh = bat * NH + head;
  const bool is_red = g < G_RED + N_RED, is_ffn = g >= G_FFN && g < G_FFN + N_FFN;
  const bool is_mod = g >= G_MOD && g < G_MOD + N_MOD, is_qp = g >= G_QP;
  const bool has_img = is_mod || is_qp || is_ffn;
  const int fi = g - G_FFN;
  const int fcg = fi < 48 ? (fi >> 4) * 16 + (fi & 7) * 2 + ((fi >> 3) & 1) : fi;
  const rsrc_t rws = mk_rsrc(ws, WS_END);
  volatile LAS u32* flags = (volatile LAS u32*)(L + L_FLAG);
  const u32 lds0 = (u32)(uintptr_t)L;
  u32 tag;
  u32 wd[32];
  s16x8 af[14];

  if (wave < 4) {
    const int s0 = spl < 3 ? spl * NSA : spl < 5 ? 3 * NSA + (spl - 3) * NSF : spl < 6 ? 3 * NSA + 2 * NSF : 3 * NSA + 2 * NSF + NSM + (spl - 6) * NSQ;
    const size_t chunk = ((size_t)bh * SK) * (DH * 2) + (size_t)s0 * 4096;
    const char* kb = (const char*)pK + chunk;
    const char* vb = (const char*)pV + chunk;
    const int half = lane >> 5, c = lane & 31;
    const int st_off = WS_ST + (bh * 8 + spl) * 128;
    f32x2 acc[4];
    float scale = 0.f;
    wg_barrier();
    if (is_qp || is_mod) lds_wait(flags + 5);
    SCHED();
    if (spl < 3) stream_kv<NSA, NPRE, WIN>(L, rws, kb, vb, wave, lane, st_off, tag, acc, scale);
    else if (spl < 5) stream_kv<NSF, (NPRE < NSF ? NPRE : NSF), WIN>(L, rws, kb, vb, wave, lane, st_off, tag, acc, scale);
    else if (spl < 6) stream_kv<NSM, (NSM < RING ? NSM : RING), WIN>(L, rws, kb, vb, wave, lane, st_off, tag, acc, scale);
    else stream_kv<NSQ, PREQ, WIN>(L, rws, kb, vb, wave, lane, st_off, tag, acc, scale);
    f32x4 o4;
    o4.x = tr32(acc[0].x, acc[2].x) * scale; o4.y = tr32(acc[0].y, acc[2].y) * scale;
    o4.z = tr32(acc[1].x, acc[3].x) * scale; o4.w = tr32(acc[1].y, acc[3].y) * scale;
    *(LAS f32x4*)(L + L_O + (wave * 256 + c * 8 + half * 4) * 4) = o4;
    wg_barrier();
    if (wave < 2) {
      const LAS f32x2* O2 = (const LAS f32x2*)(L + L_O) + tid;
      const f32x2 s = (O2[0] + O2[128]) + (O2[256] + O2[384]);
      bst128_sc1(rws, WS_O + ((bh * 8 + spl) * 128 + tid) * 16, u32x4{__float_as_uint(s.x), tag, __float_as_uint(s.y), tag});
    }
    if (is_red) {
      const int b = g / 28, cg = g % 28;
      LAS f32x2* red = (LAS f32x2*)(L + L_RED);
      const int l8 = lane & 7;
      float x1a, x1b;
      {
        int vo[4]; bool ac[4] = {true, true, true, true}; u32x4 r[4];
#pragma unroll
        for (int i = 0; i < 4; ++i) vo[i] = WS_Y + ((b * 128 + 32 * wave + 8 * i + (lane >> 3)) * 224 + cg * 8 + l8) * 16;
        poll<4>(rws, vo, ac, tag, r);
        float s0 = (__uint_as_float(r[0].x) + __uint_as_float(r[1].x)) + (__uint_as_float(r[2].x) + __uint_as_float(r[3].x));
        float s1 = (__uint_as_float(r[0].z) + __uint_as_float(r[1].z)) + (__uint_as_float(r[2].z) + __uint_as_float(r[3].z));
        s0 += dpp<ROR8>(s0); s1 += dpp<ROR8>(s1);
        s0 = tr16(s0, s0); s1 = tr16(s1, s1);
        s0 = tr32(s0, s0); s1 = tr32(s1, s1);
        if (lane < 8) red[wave * 8 + lane] = f32x2{s0, s1};
        wg_barrier();
        const f32x2 t = (red[l8] + red[8 + l8]) + (red[16 + l8] + red[24 + l8]);
        const u32 ga = ((const LAS u32*)(L + L_AUX))[l8], xx = ((const LAS u32*)(L + L_AUX + 64))[l8];
        x1a = rbf(blo(xx) + rbf(blo(ga) * rbf(t.x)));
        x1b = rbf(bhi(xx) + rbf(bhi(ga) * rbf(t.y)));
        if (wave == 0) {
          const u32 pk = pack2(x1a, x1b), nb = dppu<XOR1>(pk);
          if (lane < 8 && (lane & 1) == 0) bst128_sc1(rws, WS_X1 + g * 128 + (lane >> 1) * 16, u32x4{pk, tag, nb, tag});
        }
      }
      {
        const bool a1 = lane < 48;
        const int p0 = 14 * wave + (lane >> 3), p1 = 14 * wave + (a1 ? 8 + (lane >> 3) : 0);
        int vo[2] = {WS_F + ((p0 * 2 + b) * 224 + cg * 8 + l8) * 16, WS_F + ((p1 * 2 + b) * 224 + cg * 8 + l8) * 16};
        bool ac[2] = {true, a1}; u32x4 r[2];
        poll<2>(rws, vo, ac, tag, r);
        float s0 = __uint_as_float(r[0].x) + __uint_as_float(r[1].x);
        float s1 = __uint_as_float(r[0].z) + __uint_as_float(r[1].z);
        s0 += dpp<ROR8>(s0); s1 += dpp<ROR8>(s1);
        s0 = tr16(s0, s0); s1 = tr16(s1, s1);
        s0 = tr32(s0, s0); s1 = tr32(s1, s1);
        if (lane < 8) red[32 + wave * 8 + lane] = f32x2{s0, s1};
        wg_barrier();
        if (wave == 0 && lane < 8) {
          const f32x2 t = (red[32 + lane] + red[40 + lane]) + (red[48 + lane] + red[56 + lane]);
          const u32 gb = ((const LAS u32*)(L + L_AUX + 32))[lane];
          ((u32*)pout)[(b * D + cg * 16) / 2 + lane] = pack2(x1a + rbf(blo(gb) * rbf(t.x)), x1b + rbf(bhi(gb) * rbf(t.y)));
          if (g == 0 && lane == 0) __builtin_amdgcn_raw_buffer_store_b32(tag, rws, WS_EPOCH, 0, AUX_SC1);
        }
      }
      return;
    }
    if (!is_ffn) return;
    load_wd(L, wave & 3, lane, wd);
    {
      const int lb = wave >> 1, lh = wave & 1;
      const bool act1 = lane < 48, lact = lh == 0 || act1;
      const int q1 = act1 ? 64 + lane : 0, lrec = lh == 0 ? lane : q1;
      const u32x2 shw = *(const LAS u32x2*)(L + L_LN + lb * 896 + lrec * 8), scw = *(const LAS u32x2*)(L + L_LN + 1792 + lb * 896 + lrec * 8);
      int vo[2] = {WS_X1 + (lb * 28 + (lane >> 2)) * 128 + (lane & 3) * 16, WS_X1 + (lb * 28 + (q1 >> 2)) * 128 + (q1 & 3) * 16};
      bool ac[2] = {true, act1}; u32x4 r[2];
      poll<2>(rws, vo, ac, tag, r);
      const float xf[8] = {blo(r[0].x), bhi(r[0].x), blo(r[0].z), bhi(r[0].z), blo(r[1].x), bhi(r[1].x), blo(r[1].z), bhi(r[1].z)};
      float mean_b, rsd;
      ln_stats1(xf, act1, mean_b, rsd);
      const u32 xw0 = lh ? r[1].x : r[0].x, xw1 = lh ? r[1].z : r[0].z;
      const float h0 = rbf(rbf(rbf(blo(xw0) - mean_b) * rsd) * blo(scw.x)) + blo(shw.x), h1 = rbf(rbf(rbf(bhi(xw0) - mean_b) * rsd) * bhi(scw.x)) + bhi(shw.x);
      const float h2 = rbf(rbf(rbf(blo(xw1) - mean_b) * rsd) * blo(scw.y)) + blo(shw.y), h3 = rbf(rbf(rbf(bhi(xw1) - mean_b) * rsd) * bhi(scw.y)) + bhi(shw.y);
      if (lact) *(LAS u32x2*)(L + L_H + lb * 1024 + lrec * 8) = u32x2{pack2(h0, h1), pack2(h2, h3)};
    }
  } else {
    const int a = wave - 4;
    __builtin_amdgcn_s_setprio(3);
    u32 ep = ald32_sc1(ws, WS_EPOCH);
    const bool act1 = lane < 48;
    const int qi = g - G_QP, mi = g - G_MOD;
    const int r0 = 4 * lane, r1 = act1 ? 256 + 4 * lane : 0;
    const int b = lane >> 5, rr = lane & 31;
    const bool actr = rr < 28;
    const int rec = 28 * a + (actr ? rr : 0);
    u32x2 xa0 = {0u, 0u}, xa1 = {0u, 0u}, xb0 = {0u, 0u}, xb1 = {0u, 0u}, xm = {0u, 0u};
    u32 c0 = 0u, c1 = 0u;
    if (is_mod) {
      const int l56 = lane < 56 ? lane : 0;
      c0 = ald32(pc, (112 * a + 2 * l56) * 2); c1 = ald32(pc, (D + 112 * a + 2 * l56) * 2);
    }
    if (is_qp) {
      xa0 = ald64(px, r0 * 2); xa1 = ald64(px, r1 * 2); xb0 = ald64(px, (D + r0) * 2); xb1 = ald64(px, (D + r1) * 2);
      xm = ald64(px, (b * D + 4 * rec) * 2);
    }
    if (is_red) xm = ald64(px, ((g / 28) * D + (g % 28) * 16 + (lane & 3) * 4) * 2);
    if (a == 0 && lane == 0) { flags[0] = 0u; flags[1] = 0u; flags[3] = 0u; flags[4] = 0u; flags[5] = 0u; flags[6] = 0u; flags[7] = 0u; }
    SCHED();
    wg_barrier();
    const unsigned long long t_begin = __builtin_amdgcn_s_memrealtime();
    const int irow = 112 * a + (lane >> 3);
    const int ch0 = (lane & 7) ^ (img_swz(lane >> 3) << 1), ch1 = (lane & 7) ^ (img_swz(8 + (lane >> 3)) << 1);
    if (is_qp) {
      const char* src = (const char*)pwq + (size_t)irow * (NH * DH * 2) + qi * 128;
#pragma unroll
      for (int i = 0; i < 14; ++i) dma16(src + (size_t)i * (8 * NH * DH * 2) + ((i & 1) ? ch1 : ch0) * 16, lds0 + L_W + (112 * a + 8 * i) * 128);
      asm volatile("s_waitcnt vmcnt(14)" ::: "memory");
    } else if (is_mod) {
      const char* src = (const char*)pwmod + (size_t)(224 * (mi & 1) + 56 * a + (lane >> 3)) * (NM * 2) + (mi >> 1) * 128;
#pragma unroll
      for (int i = 0; i < 7; ++i) dma16(src + (size_t)i * (8 * NM * 2) + (((a + i) & 1) ? ch1 : ch0) * 16, lds0 + L_W + (56 * a + 8 * i) * 128);
      asm volatile("s_waitcnt vmcnt(7)" ::: "memory");
    } else {
      asm volatile("s_waitcnt vmcnt(0)" ::: "memory");
    }
    SCHED();
    asm volatile("" : "+v"(ep), "+v"(c0), "+v"(c1), "+v"(xa0), "+v"(xa1), "+v"(xb0), "+v"(xb1), "+v"(xm));
    SCHED();
    tag = __builtin_amdgcn_readfirstlane(ep) + 1u;
    if (is_mod && lane < 56) {
      const float f0 = blo(c0), f1 = bhi(c0), f2 = blo(c1), f3 = bhi(c1);
      *(LAS u32*)(L + L_H + (112 * a + 2 * lane) * 2) = pack2(f0 / (1.f + expf(-f0)), f1 / (1.f + expf(-f1)));
      *(LAS u32*)(L + L_H + 1024 + (112 * a + 2 * lane) * 2) = pack2(f2 / (1.f + expf(-f2)), f3 / (1.f + expf(-f3)));
    }
    if (is_mod) {
      DRAIN();
      aux_sync(L, 3, lane);
      load_afrags<7>(L + L_W, a, lane, af);
      const f32x4 d = gemv_tile<7>(L + L_H + (mi & 1) * 448, lane, af);
      if ((lane & 15) < 2) {
        const int ro = WS_MU + (((mi & 1) * 2 + (lane & 1)) * 448 + (mi >> 1) * 32 + a * 8 + (lane >> 4) * 2) * 16;
        bst128_sc1(rws, ro, u32x4{__float_as_uint(d.x), tag, __float_as_uint(d.y), tag});
        bst128_sc1(rws, ro + 16, u32x4{__float_as_uint(d.z), tag, __float_as_uint(d.w), tag});
      }
      aux_sync(L, 6, lane);
      if (a == 0 && lane == 0) flags[5] = 1u;
    }
    if (is_qp) {
      float mean0, sd0, mean1, sd1;
      { const float xf[8] = {blo(xa0.x), bhi(xa0.x), blo(xa0.y), bhi(xa0.y), blo(xa1.x), bhi(xa1.x), blo(xa1.y), bhi(xa1.y)}; ln_stats(xf, act1, mean0, sd0); }
      { const float xf[8] = {blo(xb0.x), bhi(xb0.x), blo(xb0.y), bhi(xb0.y), blo(xb1.x), bhi(xb1.x), blo(xb1.y), bhi(xb1.y)}; ln_stats(xf, act1, mean1, sd1); }
      const float mean_b = b ? mean1 : mean0, rsd_b = b ? sd1 : sd0;
      const float n0 = rbf(rbf(blo(xm.x) - mean_b) * rsd_b), n1 = rbf(rbf(bhi(xm.x) - mean_b) * rsd_b);
      const float n2 = rbf(rbf(blo(xm.y) - mean_b) * rsd_b), n3 = rbf(rbf(bhi(xm.y) - mean_b) * rsd_b);
      DRAIN();
      aux_sync(L, 3, lane);
      load_afrags<14>(L + L_W, a, lane, af);
      const int mb = WS_MU + (b * 448 + 2 * rec) * 16;
      int po[8] = {mb, mb + 16, mb + 224 * 16, mb + 225 * 16, mb + 896 * 16, mb + 897 * 16, mb + 1120 * 16, mb + 1121 * 16};
      bool ac[8] = {actr, actr, actr, actr, actr, actr, actr, actr};
      u32x4 r[8];
      poll<8>(rws, po, ac, tag, r);
#define FS(i, c) (__uint_as_float(r[i].c) + __uint_as_float(r[4 + (i)].c))
      const float h0 = rbf(n0 * rbf(1.f + rbf(FS(2, x)))) + rbf(FS(0, x)), h1 = rbf(n1 * rbf(1.f + rbf(FS(2, z)))) + rbf(FS(0, z));
      const float h2 = rbf(n2 * rbf(1.f + rbf(FS(3, x)))) + rbf(FS(1, x)), h3 = rbf(n3 * rbf(1.f + rbf(FS(3, z)))) + rbf(FS(1, z));
#undef FS
      if (actr) *(LAS u32x2*)(L + L_H + b * 1024 + rec * 8) = u32x2{pack2(h0, h1), pack2(h2, h3)};
      aux_sync(L, 4, lane);
      const f32x4 d = gemv_tile<14>(L + L_H, lane, af);
      if ((lane & 15) < 2)
        bst128_sc1(rws, WS_Q + (lane & 1) * (1024 * 16) + qi * 256 + (a * 4 + (lane >> 4)) * 16, u32x4{pack2(d.x, d.y), tag, pack2(d.z, d.w), tag});
      aux_sync(L, 6, lane);
      if (a == 0 && lane == 0) flags[5] = 1u;
    }
    auto late_img = [&]() {
      const char* src0 = is_mod ? (const char*)pwmod + (size_t)irow * (NM * 2) + (14 + mi) * 128 + ch0 * 16
                                : (const char*)((ch0 & 1) ? pwu : pwg) + (size_t)irow * (FF * 2) + fcg * 64 + (ch0 >> 1) * 16;
      const char* src1 = is_mod ? (const char*)pwmod + (size_t)irow * (NM * 2) + (14 + mi) * 128 + ch1 * 16
                                : (const char*)((ch1 & 1) ? pwu : pwg) + (size_t)irow * (FF * 2) + fcg * 64 + (ch1 >> 1) * 16;
      const int step = is_mod ? 8 * NM * 2 : 8 * FF * 2;
#pragma unroll
      for (int i = 0; i < 14; ++i) dma16(((i & 1) ? src1 : src0) + (size_t)i * step, lds0 + L_W + (112 * a + 8 * i) * 128);
      if (is_ffn) {
#pragma unroll
        for (int i = 0; i < 7; ++i) dma16((const char*)pwd + (size_t)fcg * 32 * (D * 2) + (a * 7 + i) * 1024 + lane * 16, lds0 + L_W2 + (a * 7 + i) * 1024);
      }
    };
    u32 wo[32];
    auto load_wo = [&]() {
      const rsrc_t rwo = mk_rsrc(pwo, NH * DH * D * 2);
      const int l56 = lane < 56 ? lane : 0;
      const int wbase = (head * DH + spl * 32) * (D * 2);
#pragma unroll
      for (int i = 0; i < 32; ++i) {
        if (i >= 8) WAIT_VM(7);
        wo[i] = bld32<AUX_NT>(rwo, (112 * a + 2 * l56) * 2, wbase + i * (D * 2));
        SCHED();
      }
#pragma unroll
      for (int p = 0; p < 16; ++p) {
        const u32 lo = perm_lo(wo[2 * p], wo[2 * p + 1]), hi = perm_hi(wo[2 * p], wo[2 * p + 1]);
        wo[2 * p] = lo; wo[2 * p + 1] = hi;
        PIN2(wo[2 * p], wo[2 * p + 1]);
      }
    };
    const bool wo_early = !has_img && a != 0;
    if (wo_early) {
      for (int i = 0; i < 4096 && __builtin_amdgcn_s_memrealtime() - t_begin < T_WO; ++i) __builtin_amdgcn_s_sleep(4);
      load_wo();
    }
    if (!is_qp && (a == 0 || is_mod || is_ffn)) {
      const unsigned lim = a == 0 ? T_QPOLL : T_IMG;
      for (int i = 0; i < 4096 && __builtin_amdgcn_s_memrealtime() - t_begin < lim; ++i) __builtin_amdgcn_s_sleep(4);
    }
    if ((is_mod || is_ffn) && a != 0) late_img();
    if (a == 0) {
      int vo[1] = {WS_Q + (bat * 1024 + head * 64 + lane) * 16};
      bool ac[1] = {true};
      u32x4 r[1];
      poll<1>(rws, vo, ac, tag, r);
      u32x2 qq = {r[0].x, r[0].z};
      *(LAS u32x2*)(L + L_Q + lane * 8) = qq;
      if (lane == 0) flags[2] = tag;
      LDS_FENCE();
      if (lane == 0) flags[0] = 1u;
      if (is_mod || is_ffn) late_img();
    }
    if (is_mod || is_ffn) {
      DRAIN();
      aux_sync(L, 7, lane);
      load_afrags<14>(L + L_W, a, lane, af);
      if (is_mod) {
        const f32x4 d = gemv_tile<14>(L + L_H, lane, af);
        if ((lane & 15) < 2)
          bst128_sc1(rws, WS_MOD + (lane & 1) * (672 * 16) + (14 + mi) * 256 + (a * 4 + (lane >> 4)) * 16, u32x4{pack2(d.x, d.y), tag, pack2(d.z, d.w), tag});
      }
    }
    if (a != 0) lds_wait(flags + 0);
    if (!wo_early) load_wo();
    if (is_red && a == 1) {
      const int rb = g / 28, rcg = g % 28;
      int vo[1] = {WS_MOD + (rb * 672 + ((lane & 4) ? 560 : 224) + rcg * 4 + (lane & 3)) * 16};
      bool ac[1] = {lane < 8};
      u32x4 r[1];
      poll<1, 8>(rws, vo, ac, tag, r);
      if (lane < 8) *(LAS u32x2*)(L + L_AUX + lane * 8) = u32x2{r[0].x, r[0].z};
      if (lane < 4) *(LAS u32x2*)(L + L_AUX + 64 + lane * 8) = xm;
    }
    if (is_ffn) {
      const int lb = a >> 1, lh = a & 1;
      const bool lact = lh == 0 || act1;
      const int lrec = lh == 0 ? lane : (act1 ? 64 + lane : 0);
      int vo[2] = {WS_MOD + (lb * 672 + 336 + lrec) * 16, WS_MOD + (lb * 672 + 448 + lrec) * 16};
      bool ac[2] = {lact, lact};
      u32x4 md[2];
      poll<2, 8>(rws, vo, ac, tag, md);
      if (lact) {
        *(LAS u32x2*)(L + L_LN + lb * 896 + lrec * 8) = u32x2{md[0].x, md[0].z};
        *(LAS u32x2*)(L + L_LN + 1792 + lb * 896 + lrec * 8) = u32x2{pack2(1.f + blo(md[1].x), 1.f + bhi(md[1].x)), pack2(1.f + blo(md[1].z), 1.f + bhi(md[1].z))};
      }
      load_wd(L, a, lane, wd);
    }
    wg_barrier();
    {
      float wgt;
      {
        int vo[1] = {WS_ST + (bh * 8 + (lane >> 3)) * 128};
        bool ac[1] = {true};
        u32x4 r[1];
        poll<1>(rws, vo, ac, tag, r);
        const float ms = __uint_as_float(r[0].x), ls = __uint_as_float(r[0].z);
        const float M = max32(max16(fmaxf(ms, dpp<ROR8>(ms))));
        const float e = __expf(ms - M);
        float t = e * ls;
        t += dpp<ROR8>(t); t = tr16(t, t); t = tr32(t, t);
        wgt = e / t;
        asm volatile("" : "+v"(wgt));
      }
      const int ob = WS_O + ((bh * 8 + (lane >> 3)) * 128 + spl * 16 + (lane & 7)) * 16;
      int vo[2] = {ob, ob + 128};
      bool ac[2] = {true, true};
      u32x4 r[2];
      poll<2>(rws, vo, ac, tag, r);
      float s0 = __uint_as_float(r[0].x) * wgt, s1 = __uint_as_float(r[0].z) * wgt, s2 = __uint_as_float(r[1].x) * wgt, s3 = __uint_as_float(r[1].z) * wgt;
      s0 += dpp<ROR8>(s0); s1 += dpp<ROR8>(s1); s2 += dpp<ROR8>(s2); s3 += dpp<ROR8>(s3);
      s0 = tr16(s0, s2); s1 = tr16(s1, s3);
      s0 = tr32(s0, s0); s1 = tr32(s1, s1);
      const u32 op = pack2(s0, s1);
      float y0 = 0.f, y1 = 0.f, y2 = 0.f, y3 = 0.f;
#pragma unroll
      for (int p = 0; p < 8; ++p) {
        const u32 oa = (u32)__builtin_amdgcn_readlane((int)op, p), oc = (u32)__builtin_amdgcn_readlane((int)op, 16 + p);
        y0 = dot2(wo[2 * p], oa, y0); y1 = dot2(wo[2 * p + 1], oa, y1);
        y2 = dot2(wo[16 + 2 * p], oc, y2); y3 = dot2(wo[16 + 2 * p + 1], oc, y3);
      }
      if (lane < 56) bst128_sc1(rws, WS_Y + ((bat * 128 + head * 8 + spl) * 224 + 56 * a + lane) * 16, u32x4{__float_as_uint(y0 + y2), tag, __float_as_uint(y1 + y3), tag});
    }
    if (!is_ffn) return;
  }

  {
    const int db = wave >> 2, dq = wave & 3;
    wg_barrier();
    if (wave >= 4) {
      const f32x4 d = gemv_tile<14>(L + L_H, lane, af);
      float ak[4];
#pragma unroll
      for (int i = 0; i < 4; ++i) {
        auto s = __builtin_amdgcn_permlane32_swap(__float_as_uint(d[i]), __float_as_uint(d[i]), false, false);
        const float gg = rbf(__uint_as_float(s[0])), uu = rbf(__uint_as_float(s[1]));
        const float z = 0.7978845608028654f * (gg + 0.044715f * gg * gg * gg);
        const float th = 1.f - 2.f * __builtin_amdgcn_rcpf(__expf(2.f * z) + 1.f);
        ak[i] = rbf(0.5f * gg * (1.f + th)) * uu;
      }
      if (lane < 32 && (lane & 15) < 2)
        *(LAS u32x2*)(L + L_ACT + (lane & 1) * 64 + (8 * (wave - 4) + 4 * (lane >> 4)) * 2) = u32x2{pack2(ak[0], ak[1]), pack2(ak[2], ak[3])};
    }
    wg_barrier();
    const LAS u32x4* ap = (const LAS u32x4*)(L + L_ACT + db * 64);
    const u32x4 a0 = ap[0], a1 = ap[1], a2 = ap[2], a3 = ap[3];
    const u32 av[16] = {a0.x, a0.y, a0.z, a0.w, a1.x, a1.y, a1.z, a1.w, a2.x, a2.y, a2.z, a2.w, a3.x, a3.y, a3.z, a3.w};
    float y0 = 0.f, y1 = 0.f, y2 = 0.f, y3 = 0.f;
#pragma unroll
    for (int p = 0; p < 8; ++p) {
      y0 = dot2(wd[2 * p], av[p], y0); y1 = dot2(wd[2 * p + 1], av[p], y1);
      y2 = dot2(wd[16 + 2 * p], av[8 + p], y2); y3 = dot2(wd[16 + 2 * p + 1], av[8 + p], y3);
    }
    if (lane < 56) bst128_sc1(rws, WS_F + ((fi * 2 + db) * 224 + 56 * dq + lane) * 16, u32x4{__float_as_uint(y0 + y2), tag, __float_as_uint(y1 + y3), tag});
  }
}

extern "C" void kernel_launch(void* const* d_in, const int* in_sizes, int n_in,
                              void* d_out, int out_size, void* d_ws, size_t ws_size,
                              hipStream_t stream) {
  fused_decode_kernel<<<NWG, NTHR, 0, stream>>>(d_in[1], d_in[2], d_ws, d_in[0], d_in[9], d_in[10],
                                                d_in[3], d_in[4], d_in[6], d_in[7], d_in[8], d_out);
}
```
